```python
import torch

def _softmax(x, axis=-1):
    return torch.softmax(x, dim=axis)

def _gelu(x):
    return torch.nn.functional.gelu(x, approximate='tanh')

def _silu(x):
    return torch.nn.functional.silu(x)

def _sigmoid(x):
    return torch.sigmoid(x)

def _layernorm(x, axis=-1, eps=1e-05):
    m = x.mean(dim=axis, keepdim=True)
    v = x.var(dim=axis, keepdim=True, unbiased=False)
    return (x - m) / torch.sqrt(v + eps)

def _scatter_add(tgt, idx, src, axis=0):
    return tgt.clone().index_add_(axis, idx, src)

def _segment_sum(src, seg, n):
    out = torch.zeros((n,) + src.shape[1:], dtype=src.dtype)
    return out.index_add_(0, seg, src)

def _pad(x, pw):
    flat = tuple((v for pair in reversed(pw) for v in pair))
    return torch.nn.functional.pad(x, flat)
kernel_name = 'concat_heads__memory_aug_recurrent_mem_tr__mha_block__prompt'

def setup_inputs():

    _w = lambda a: (lambda t: t.to(torch.bfloat16) if t.is_floating_point() else t)(torch.as_tensor(a))

    def _A_setup():
        B, S, Dm = (1, 2048, 1024)
        H, Dh = (32, 128)
        M, seg = (8, 32)
        seg = min(seg, S)
        S = S // seg * seg
        return {'x': _w((lambda *s: torch.randn(*s, dtype=torch.bfloat16) * 0.05)(B, S, Dm)), 'mem0': _w((lambda *s: torch.randn(*s, dtype=torch.bfloat16) * 1.6)(B, M, Dm)), 'Wq': _w((lambda *s: torch.randn(*s, dtype=torch.bfloat16) * 0.8)(Dm, H, Dh) * Dm ** (-0.5)), 'Wk': _w((lambda *s: torch.randn(*s, dtype=torch.bfloat16) * 0.8)(Dm, H, Dh) * Dm ** (-0.5)), 'Wv': _w((lambda *s: torch.randn(*s, dtype=torch.bfloat16) * 1.6)(Dm, H, Dh) * Dm ** (-0.5)), 'Wo': _w((lambda *s: torch.randn(*s, dtype=torch.bfloat16) * 1.6)(H, Dh, Dm) * (H * Dh) ** (-0.5)), 'seg_len': seg}
    _A_in = _A_setup()

    def _B_setup():
        B, S, D, H = (1, 2048, 1024, 32)
        return {'x': _w((lambda *s: torch.randn(*s, dtype=torch.bfloat16) * 0.2)(B, S, D)), 'wqkv': _w((lambda *s: torch.randn(*s, dtype=torch.bfloat16) * 0.2)(D, 3 * D)), 'wo': _w((lambda *s: torch.randn(*s, dtype=torch.bfloat16) * 0.05)(D, D))}
    _B_in = _B_setup()
    return {'A_x': _A_in['x'], 'A_mem0': _A_in['mem0'], 'A_Wq': _A_in['Wq'], 'A_Wk': _A_in['Wk'], 'A_Wv': _A_in['Wv'], 'A_Wo': _A_in['Wo'], 'A_seg_len': _A_in['seg_len'], 'B_x': _B_in['x'], 'B_wqkv': _B_in['wqkv'], 'B_wo': _B_in['wo']}

def reference(A_x, A_mem0, A_Wq, A_Wk, A_Wv, A_Wo, A_seg_len, B_x, B_wqkv, B_wo):

    def _A_ref(x, mem0, Wq, Wk, Wv, Wo, seg_len):
        B, S, Dm = x.shape
        H, Dh = (Wq.shape[1], Wq.shape[2])
        M = mem0.shape[1]
        scale = Dh ** (-0.5)
        n_seg = S // seg_len
        L = M + seg_len
        idx = torch.arange(L)
        mask = (idx[:, None] < M) | (idx[:, None] >= idx[None, :])
        mem = mem0
        outs = []
        for s in range(n_seg):
            xs = x[:, s * seg_len:(s + 1) * seg_len, :].clone()
            h = torch.concatenate([mem, xs], axis=1)
            q = torch.einsum('btd,dhe->bhte', h, Wq)
            k = torch.einsum('btd,dhe->bhte', h, Wk)
            v = torch.einsum('btd,dhe->bhte', h, Wv)
            scores = torch.einsum('bhqe,bhke->bhqk', q, k) * scale
            scores = torch.where(mask[None, None], scores, -1000000000.0)
            p = _softmax(scores, axis=-1)
            o = torch.einsum('bhqk,bhke->bhqe', p, v)
            o = torch.einsum('bhte,hed->btd', o, Wo)
            mem = o[:, :M, :].clone()
            outs.append(o[:, M:, :])
        return torch.concatenate(outs, axis=1)

    def _B_ref(x, wqkv, wo):
        B, S, D = x.shape
        H = 32
        Dh = D // H
        qkv = (lambda x, *d: x.permute(*d))((x @ wqkv).reshape(B, S, 3, H, Dh), 2, 0, 3, 1, 4)
        q, k, v = (qkv[0], qkv[1], qkv[2])
        scores = torch.einsum('bhqd,bhkd->bhqk', q, k) * Dh ** (-0.5)
        p = _softmax(scores, axis=-1)
        o = torch.einsum('bhqk,bhkd->bhqd', p, v)
        return (lambda x, *d: x.permute(*d))(o, 0, 2, 1, 3).reshape(B, S, D) @ wo
    a = _A_ref(A_x, A_mem0, A_Wq, A_Wk, A_Wv, A_Wo, A_seg_len)
    b = _B_ref(B_x, B_wqkv, B_wo)
    return torch.concatenate([a, b], axis=-1)
```

```cpp
#include <hip/hip_runtime.h>
#include <hip/hip_bf16.h>
#pragma clang diagnostic ignored "-Wint-to-pointer-cast"
#define LAS __attribute__((address_space(3)))
#define DEVI __device__ __forceinline__
typedef unsigned v4u __attribute__((ext_vector_type(4)));
typedef unsigned v2u __attribute__((ext_vector_type(2)));
typedef unsigned v8u __attribute__((ext_vector_type(8)));
typedef int v4i __attribute__((ext_vector_type(4)));
typedef float f4 __attribute__((ext_vector_type(4)));
typedef float f2 __attribute__((ext_vector_type(2)));
typedef short s4 __attribute__((ext_vector_type(4)));
typedef __bf16 bf8 __attribute__((ext_vector_type(8)));
typedef __bf16 bf16x16 __attribute__((ext_vector_type(16)));
typedef __bf16 bf2 __attribute__((ext_vector_type(2)));
typedef __amdgpu_buffer_rsrc_t rsrc_t;
typedef LAS unsigned char* lptr;

constexpr size_t MiB = 1u << 20;
constexpr unsigned WS_Y = 0;
constexpr unsigned WS_SM = 64 * MiB;
constexpr unsigned WS_U = 72 * MiB;
constexpr unsigned WS_V = 200 * MiB;
constexpr unsigned WS_KM = 216 * MiB;
constexpr unsigned WS_PX = 220 * MiB;
constexpr unsigned WS_PM = 224 * MiB;
constexpr unsigned WS_BQ = 225 * MiB, WS_BK = 229 * MiB, WS_BV = 233 * MiB, WS_BO = 237 * MiB;
constexpr unsigned WS_CTL = 241 * MiB;
constexpr unsigned WS_DBG = 242 * MiB;
constexpr unsigned WS_END = 244 * MiB;
constexpr int C_ABORT = 0, C_CAL = 1, C_PROG = 2, C_TICKET = 3, C_XT = 4  , C_FK = 8  , C_FQ = 264  , C_FV = 520  ,
              C_CU = 776  , C_BKV = 784  , C_BQ = 792  , C_BO = 856  , C_CP = 864  , C_LAST = 928, C_ARR = 929, C_FB = 930, C_NWORDS = 931;
constexpr unsigned N_X = 4 * MiB, N_W = 8 * MiB, N_BW = 6 * MiB, N_BWO = 2 * MiB, N_OUT = 8 * MiB, N_MEM0 = 16384;
constexpr unsigned SENT = 0xFFFFFFFFu;
constexpr int SC1 = 16;
constexpr int LDS_BYTES = 160 * 1024;

DEVI rsrc_t mkrs(const void* p, unsigned n) { return __builtin_amdgcn_make_buffer_rsrc((void*)p, 0, n, 0x00020000); }
constexpr int OOB = (int)0x80000000u;
DEVI v4i mkrs_s(const void* p, unsigned n) { unsigned long long a = (unsigned long long)p; v4i r; r.x = (int)(unsigned)a; r.y = (int)(unsigned)(a >> 32) & 0xffff; r.z = (int)n; r.w = 0x00020000; return r; }
DEVI int rfl(int v) { return __builtin_amdgcn_readfirstlane(v); }
DEVI unsigned long long clk() { return __builtin_amdgcn_s_memtime(); }
DEVI unsigned cvtpk(float lo, float hi) { bf2 r; r.x = (__bf16)lo; r.y = (__bf16)hi; return __builtin_bit_cast(unsigned, r); }
DEVI v4u ld16(rsrc_t r, int vo, int so) { return __builtin_amdgcn_raw_buffer_load_b128(r, vo, so, SC1); }
DEVI void st16(v4u v, rsrc_t r, int vo, int so) { __builtin_amdgcn_raw_buffer_store_b128(v, r, vo, so, SC1); asm volatile("s_nop 1" :: "v"(v) : "memory"); }
DEVI void st8(v2u v, rsrc_t r, int vo, int so) { __builtin_amdgcn_raw_buffer_store_b64(v, r, vo, so, SC1); }
DEVI void st4(unsigned v, rsrc_t r, int vo, int so) { __builtin_amdgcn_raw_buffer_store_b32(v, r, vo, so, SC1); }
DEVI unsigned umax3(unsigned a, unsigned b, unsigned c) { return max(max(a, b), c); }
DEVI unsigned mx4(v4u v) { return max(umax3(v.x, v.y, v.z), v.w); }
template <bool COH> DEVI void dma16(v4i rs, unsigned lds, int vo, int so) {
  if (COH) asm volatile("s_mov_b32 m0, %0\n\ts_nop 0\n\tbuffer_load_dwordx4 %1, %2, %3 offen sc1 lds" :: "s"(lds), "v"(vo), "s"(rs), "s"(so) : "memory");
  else asm volatile("s_mov_b32 m0, %0\n\ts_nop 0\n\tbuffer_load_dwordx4 %1, %2, %3 offen lds" :: "s"(lds), "v"(vo), "s"(rs), "s"(so) : "memory");
}
DEVI void warm4(v4i rs, unsigned lds, int vo, int so) { asm volatile("s_mov_b32 m0, %0\n\ts_nop 0\n\tbuffer_load_dword %1, %2, %3 offen sc1 lds" :: "s"(lds), "v"(vo), "s"(rs), "s"(so) : "memory"); }
#define WAITV(n) asm volatile("s_waitcnt vmcnt(" #n ")" ::: "memory")
#define WAITL(n) asm volatile("s_waitcnt lgkmcnt(" #n ")" ::: "memory")
#define BARRIER() asm volatile("s_waitcnt lgkmcnt(0)\n\ts_barrier" ::: "memory")
DEVI v4u lds16(lptr p, int off) { return *(LAS v4u*)(p + off); }
DEVI v2u ldstr(lptr p, int off) { return __builtin_bit_cast(v2u, __builtin_amdgcn_ds_read_tr16_b64_v4i16((LAS s4*)(p + off))); }
DEVI f4 mfma16(v4u a, v4u b, f4 c) { return __builtin_amdgcn_mfma_f32_16x16x32_bf16(__builtin_bit_cast(bf8, a), __builtin_bit_cast(bf8, b), c, 0, 0, 0); }
DEVI f4 smf(v4u a, v8u b, f4 c, int idx) { return __builtin_amdgcn_smfmac_f32_16x16x64_bf16(__builtin_bit_cast(bf8, a), __builtin_bit_cast(bf16x16, b), c, idx, 0, 0); }
DEVI v8u cat8(v4u a, v4u b) { return __builtin_shufflevector(a, b, 0, 1, 2, 3, 4, 5, 6, 7); }
DEVI unsigned fb(float f) { return __builtin_bit_cast(unsigned, f); }
DEVI float bf(unsigned u) { return __builtin_bit_cast(float, u); }
DEVI unsigned ctl_ld(unsigned* ctl, int w) { return __hip_atomic_load(ctl + w * 32, __ATOMIC_RELAXED, __HIP_MEMORY_SCOPE_AGENT); }
DEVI void ctl_st(unsigned* ctl, int w, unsigned v) { __hip_atomic_store(ctl + w * 32, v, __ATOMIC_RELAXED, __HIP_MEMORY_SCOPE_AGENT); }
DEVI void ctl_add(unsigned* ctl, int w, unsigned v) { __hip_atomic_fetch_add(ctl + w * 32, v, __ATOMIC_RELAXED, __HIP_MEMORY_SCOPE_AGENT); }
constexpr unsigned SPIN_CAP = 1u << 20;
DEVI bool ctl_wait(unsigned* ctl, int w, unsigned want) {
#pragma clang loop unroll(disable)
  for (unsigned sp = 0;; ++sp) {
    if ((unsigned)rfl(ctl_ld(ctl, w)) >= want) return true;
    __builtin_amdgcn_s_sleep(4);
    if ((sp & 63u) == 63u && rfl(ctl_ld(ctl, C_ABORT))) return false;
    if (sp > SPIN_CAP) { ctl_st(ctl, C_ABORT, 1u); return false; }
  }
}
DEVI int xcc_id() { return (int)(__builtin_amdgcn_s_getreg((3 << 11) | 20) & 0xFu); }
struct Params {
  const unsigned char *x, *mem0, *Wq, *Wk, *Wv, *Wo, *bx, *bwqkv, *bwo;
  unsigned char *out, *ws;
  int d1, d2, d3, d4, d5, mode;
};
#define D1 700
#define D2 1400
#define D3 500
#define D4 600
#define D5 200
#define KMODE 0
#define DBG 0
namespace chain {
constexpr int NRF = 24;
constexpr int L_MEM = 0, L_KX = 16384, L_QT = 24576, L_KT = 26624, L_RED = 28672, L_WL = 32768, L_JUNK = L_RED;
template <int N> DEVI void waitv() { asm volatile("s_waitcnt vmcnt(%0)" :: "n"(N) : "memory"); }
constexpr int slot_of(int f) { return (f + 8) % 10; }
DEVI int wtr_off(int w, int lane) {
  const int g = lane >> 4, a = (lane >> 2) & 3, k = 16 * (g >> 1) + 4 * (g & 1) + (a & 1) + 8 * (a >> 1), fk = (k & 1) | (((k >> 2) & 3) << 1);
  return k * 256 + ((w ^ fk) << 5) + (lane & 3) * 8;
}
DEVI v8u wtr(lptr p, int off) {
  const v2u t0 = ldstr(p, off), t1 = ldstr(p, off + 512), t2 = ldstr(p, off + 8192), t3 = ldstr(p, off + 8704);
  return v8u{t0.x, t0.y, t1.x, t1.y, t2.x, t2.y, t3.x, t3.y};
}

template <bool ISA> DEVI void sub_src(const Params& p, int f, int h, v4i& rs, int& so) {
  if (ISA) { rs = mkrs_s((f & 1) ? p.Wk : p.Wq, N_W); so = ((f >> 1) * 64 * 4096 + h * 128) * 2; }
  else if (f < 16) { rs = mkrs_s(p.Wv, N_W); so = (f * 64 * 4096 + h * 128) * 2; }
  else { rs = mkrs_s(p.Wo, N_W); so = ((h * 128 + (f & 1) * 64) * 1024 + ((f - 16) >> 1) * 128) * 2; }
}
template <bool ISA, int F> DEVI void issue_sub(const Params& p, int h, int w, int lane, unsigned lds0) {
  v4i rs; int so; sub_src<ISA>(p, F, h, rs, so);
  const int stride = (ISA || F < 16) ? 8192 : 2048;
#pragma unroll
  for (int i = 0; i < 2; ++i) {
    const int k = (w * 2 + i) * 4 + (lane >> 4), fk = (k & 1) | (((k >> 2) & 3) << 1);
    dma16<false>(rs, lds0 + slot_of(F) * 16384 + (w * 2 + i) * 1024, k * stride + (((lane & 15) ^ (fk << 1)) << 4), so);
  }
}
template <bool ISA, int F> DEVI void load_step(const Params& p, int h, int w, int lane, lptr Lt, unsigned lds0, v8u (&W)[NRF]) {
  waitv<2 * (F + 8 < 31 ? 8 : 31 - F)>();
  BARRIER();
  if constexpr (F + 9 < 32) issue_sub<ISA, F + 9>(p, h, w, lane, lds0);
  lptr q = Lt; asm volatile("" : "+v"(q)); q += slot_of(F) * 16384; asm volatile("" : "+v"(q));
  W[F] = wtr(q, 0);
  if constexpr (F + 1 < NRF) load_step<ISA, F + 1>(p, h, w, lane, Lt, lds0, W);
}
template <bool ISA, int F> DEVI void issue_pre(const Params& p, int h, int w, int lane, unsigned lds0) {
  issue_sub<ISA, F>(p, h, w, lane, lds0);
  if constexpr (F + 1 < 9) issue_pre<ISA, F + 1>(p, h, w, lane, lds0);
}
DEVI void spin_until(unsigned t0, int d) {
#pragma clang loop unroll(disable)
  while ((unsigned)clk() - t0 < (unsigned)d) {}
}
DEVI float vmax(float a, float b) { float r; asm("v_max_f32 %0, %1, %2" : "=v"(r) : "v"(a), "v"(b)); return r; }
DEVI float vmax3(float a, float b, float c) { float r; asm("v_max3_f32 %0, %1, %2, %3" : "=v"(r) : "v"(a), "v"(b), "v"(c)); return r; }
DEVI float xmax(float v) {
  asm("s_nop 1\n\tv_max_f32_dpp %0, %0, %0 row_ror:8 row_mask:0xf bank_mask:0xf" : "+v"(v));
  auto a = __builtin_amdgcn_permlane16_swap(fb(v), fb(v), false, false); v = vmax(bf(a[0]), bf(a[1]));
  auto b = __builtin_amdgcn_permlane32_swap(fb(v), fb(v), false, false); return vmax(bf(b[0]), bf(b[1]));
}
DEVI float xsum(float v) {
  v += bf(__builtin_amdgcn_update_dpp(0u, fb(v), 0x128, 0xf, 0xf, false));
  auto a = __builtin_amdgcn_permlane16_swap(fb(v), fb(v), false, false); v = bf(a[0]) + bf(a[1]);
  auto b = __builtin_amdgcn_permlane32_swap(fb(v), fb(v), false, false); return bf(b[0]) + bf(b[1]);
}
DEVI int sm_base(int s, int c) { return (int)WS_SM + s * 131072 + ((((s >> 4) ^ c) & 1) << 13); }
DEVI int y_base(int s, int c) { return (int)WS_Y + s * 1048576 + ((((s >> 1) ^ c) & 1) << 13); }
DEVI int kq_base(int s, int c, int h, int which) { return (y_base(s, c) ^ 8192) + (h * 2 + which) * 16384; }
constexpr int SM_PX = 32768, SM_PM = 65536;

DEVI unsigned rtime() { unsigned long long t; asm volatile("s_memrealtime %0\n\ts_waitcnt lgkmcnt(0)" : "=s"(t) :: "memory"); return (unsigned)t; }
DEVI unsigned ctime() { unsigned long long t; asm volatile("s_memtime %0\n\ts_waitcnt lgkmcnt(0)" : "=s"(t) :: "memory"); return (unsigned)t; }
DEVI int lane_now() { unsigned z = 0; asm volatile("" : "+s"(z)); return (int)__builtin_amdgcn_mbcnt_hi(~0u, __builtin_amdgcn_mbcnt_lo(~0u, z)); }
DEVI int mkidx(int lane) { return 0x4444 + ((lane >> 3) & 1) * 0xAAAA; }
DEVI v4u mkones() { unsigned long long z = ~0ull; asm volatile("" : "+s"(z)); const v2u a = __builtin_bit_cast(v2u, z); return v4u{a.x, a.y, a.x, a.y}; }
DEVI f4 mkz2() { unsigned long long z = 0; asm volatile("" : "+s"(z)); const f2 a = __builtin_bit_cast(f2, z); return f4{a.x, a.y, a.x, a.y}; }
#define PIN(x) asm volatile("" : "+v"(x))
#define SB() __builtin_amdgcn_sched_barrier(0)
constexpr int a_order(int i) { return (i & 3) == 3 ? 12 + (i >> 2) : i - (i >> 2); }
DEVI v8u wl(lptr p, int off) { return cat8(lds16(p, off), lds16(p, off + 1024)); }
template <int I, class F> DEVI void a_gemm(const v8u (&W)[NRF], lptr Lm, lptr Lw0, lptr Lw1, v4u (&a)[4], v8u& wq, v8u& wk, f4& aq, f4& ak, int idx, F&& side) {
  constexpr int kb = a_order(I);
  if constexpr (kb < 12) { aq = smf(a[I & 3], W[2 * kb], aq, idx); ak = smf(a[I & 3], W[2 * kb + 1], ak, idx); }
  else { aq = smf(a[I & 3], wq, aq, idx); ak = smf(a[I & 3], wk, ak, idx); }
  SB();
  if constexpr (I + 4 < 16) a[I & 3] = lds16(Lm, a_order(I + 4) * 1024);
  if constexpr (I == 3) wq = wl(Lw0, 32768);
  if constexpr (I == 4) wk = wl(Lw0, 49152);
  if constexpr (I == 7) wq = wl(Lw1, 0);
  if constexpr (I == 8) wk = wl(Lw1, 16384);
  if constexpr (I == 11) wq = wl(Lw1, 32768);
  if constexpr (I == 12) wk = wl(Lw1, 49152);
  if constexpr (I == 1) side();
  SB();
  if constexpr (I + 1 < 16) a_gemm<I + 1>(W, Lm, Lw0, Lw1, a, wq, wk, aq, ak, idx, side);
}
template <int I, class F> DEVI void v_gemm(const v8u (&W)[NRF], lptr Lm, v4u (&a)[4], f4& av, int idx, F&& side) {
  av = smf(a[I & 3], W[I], av, idx);
  SB();
  if constexpr (I + 4 < 16) a[I & 3] = lds16(Lm, (I + 4) * 1024);
  if constexpr (I == 2) side();
  SB();
  if constexpr (I + 1 < 16) v_gemm<I + 1>(W, Lm, a, av, idx, side);
}
DEVI v2u fold2(f4 a, f4 b) {
  float f[4];
#pragma unroll
  for (int r = 0; r < 4; ++r) { auto t = __builtin_amdgcn_permlane32_swap(fb(a[r]), fb(b[r]), false, false); f[r] = bf(t[0]) + bf(t[1]); }
  return v2u{cvtpk(f[0], f[1]), cvtpk(f[2], f[3])};
}
#define PINS(x) asm volatile("" : "+s"(x))

template <bool ISA> DEVI void run(const Params& p, int role, lptr L) {
  const int w = rfl(threadIdx.x >> 6), h = role & 31, o = role;
  const unsigned lds0 = (unsigned)(unsigned long long)L;
  unsigned* ctl = (unsigned*)(p.ws + WS_CTL);
  const rsrc_t ws = mkrs(p.ws, WS_END); const v4i wss = mkrs_s(p.ws, WS_END);
  const unsigned rts = rtime();
  v8u W[NRF];
  { const int lane = lane_now();
    const lptr Lt = L + wtr_off(w, lane);
    issue_pre<ISA, 0>(p, h, w, lane, lds0);
    load_step<ISA, 0>(p, h, w, lane, Lt, lds0, W);
    WAITV(0);
#pragma unroll
    for (int half = 0; half < 4; ++half) {
      if (half == 0) BARRIER();
      v8u t[2];
      lptr q = Lt; PIN(q); q += L_WL + half * 32768; PIN(q);
#pragma unroll
      for (int k = 0; k < 2; ++k) t[k] = wtr(q, k * 16384);
      BARRIER();
      lptr d = L + w * 2048 + lane * 16; PIN(d); d += L_WL + half * 32768; PIN(d);
#pragma unroll
      for (int k = 0; k < 2; ++k) { *(LAS v4u*)(d + k * 16384) = __builtin_shufflevector(t[k], t[k], 0, 1, 2, 3); *(LAS v4u*)(d + k * 16384 + 1024) = __builtin_shufflevector(t[k], t[k], 4, 5, 6, 7); }
    }
  }
  if (!ctl_wait(ctl, C_CAL, 1u)) return;
  const int c = (int)(rfl(ctl_ld(ctl, C_CAL)) & 1);
  v8u PA; v4u PB;
  {
    int l16, idx, x1, x2, pm2, mo, kxo; lptr Le, Lq, Lr, Lr2;
    const int lane = lane_now(), g = lane >> 4;
    l16 = lane * 16; idx = mkidx(lane);
    Le = L + L_QT + (w * 16 + (lane & 15)) * 16 + (g & 1) * 8 + (lane >> 5) * (ISA ? 2048 : 4);
    Lq = ISA ? L + L_QT + (4 * g + ((lane >> 2) & 3)) * 16 + (lane & 1) * 8 : L + L_QT + (16 * g + 8 * ((lane >> 1) & 1) + ((lane >> 2) & 3)) * 16 + (lane & 1) * 8;
    const int pxb = SM_PX + (h >> 4) * 16384 + (h & 15) * 512, pmb = SM_PM + h * 128, mb = (o >> 5) * 16384 + (o & 31) * 256;
    x2 = (lane & 8) ? OOB : pmb + (lane & 7) * 16 + (g & 1) * 8 + (g >> 1) * 4;
    if (w == 0) x1 = pxb + (lane & 7) * 16 + (g & 1) * 8 + ((lane >> 3) & 1) * 256 + (lane >> 5) * 128;
    else if (w == 1) x1 = lane < 16 ? mb + l16 : !ISA ? OOB : lane < 48 ? pxb + l16 - 256 : lane < 56 ? pmb + l16 - 768 : OOB;
    else x1 = (lane & 8) ? OOB : h * 2048 + (w & 3) * 512 + g * 128 + (lane & 7) * 16;
    pm2 = (lane & 8) ? OOB : SM_PM + w * 512 + g * 128 + (lane & 7) * 16;
    Lr = L + L_RED + w * 512 + (g & 1) * 256 + (lane >> 5) * 128 + (lane & 15) * 4;
    Lr2 = L + L_RED + (lane >> 5) * 2048 + (lane & 31) * 16;
    mo = mb + ((lane >> 1) & 1) * 128 + ((lane >> 2) & 7) * 16 + (lane & 1) * 8 + (lane >> 5) * 4;
    const int rho = lane & 15, tau = 16 * ((rho >> 1) & 1) + 8 * (w >> 2) + 4 * ((rho >> 2) & 1) + 2 * (rho >> 3) + (rho & 1);
    kxo = ISA ? (h * 2 + 1) * 16384 + (tau >> 4) * 4096 + (w & 3) * 1024 + (g * 16 + (tau & 15)) * 16
              : ((g & 1) + (lane >> 5) * 4) * 131072 + w * 16384 + h * 256 + (lane & 15) * 16;
    PA = v8u{(unsigned)l16, (unsigned)idx, (unsigned)x1, (unsigned)x2, (unsigned)pm2, (unsigned)mo, (unsigned)kxo, (unsigned)(unsigned long long)Le};
    PB = v4u{(unsigned)(unsigned long long)Lq, (unsigned)(unsigned long long)Lr, (unsigned)(unsigned long long)Lr2, 0u};
    PIN(PA); PIN(PB);
  }
#define l16 ((int)PA[0])
#define idx ((int)PA[1])
#define x1 ((int)PA[2])
#define x2 ((int)PA[3])
#define pm2 ((int)PA[4])
#define mo ((int)PA[5])
#define kxo ((int)PA[6])
#define Le ((lptr)PA[7])
#define Lq ((lptr)PB[0])
#define Lr ((lptr)PB[1])
#define Lr2 ((lptr)PB[2])
#define Lm ((lptr)PA[0])
  const int wq4 = (w >> 2) * 16384 + (w & 3) * 2048, oobw = w < 2 ? 0 : OOB;
  int sb = sm_base(0, c), yb = y_base(0, c), su = (int)WS_U + (o * 32 + w * 4) * 1024;
  if (ISA) { if (!ctl_wait(ctl, C_FK + h, 1u)) return; dma16<true>(wss, lds0 + L_KX + w * 1024, kxo, yb ^ 8192); }
  if (!ctl_wait(ctl, C_CU, 256u)) return;
  const unsigned rt0 = rtime();
  unsigned tref = (unsigned)clk();
  unsigned np1 = 0, npt = 0, tz = 0, acc_t = 0; const int site = p.mode >> 8;
#define STAMP(k) if (DBG) { if (site == k && s >= 8 && s < 60) acc_t += ctime() - tz; }
  const int d2 = ISA ? p.d2 : p.d2 + p.d3;
  int sprev = 0;
  bool ok = true;
#pragma clang loop unroll(disable)
  for (int s = 0;; ++s) {
    unsigned np = 0;
    PIN(PA); PIN(PB);
    lptr Ls; { int t = l16; PIN(t); Ls = (lptr)(unsigned)(t + w * 2048); }
    v8u wq = wl(Ls, L_WL), wk = wl(Ls, L_WL + 16384);
    lptr Lw0 = Ls + L_WL, Lw1 = Lw0 + 65536; PIN(Lw0); PIN(Lw1);
    {
      v4u m0, m1;
      if (__builtin_expect(s == 0, 0)) {
        int t = l16; PIN(t); const int lane = t >> 4; const rsrc_t r0 = mkrs(p.mem0, N_MEM0); const int vo = ((lane & 7) * 1024 + w * 128 + (lane >> 3) * 8) * 2;
        m0 = __builtin_amdgcn_raw_buffer_load_b128(r0, vo, 0, 0); m1 = __builtin_amdgcn_raw_buffer_load_b128(r0, vo + 128, 0, 0);
        WAITV(0);
      } else {
        int so = sprev + wq4, so2 = so + 1024; PINS(so); PINS(so2);
        STAMP(10)
        spin_until(tref, p.d1);
        STAMP(13)
#pragma clang loop unroll(disable)
        for (unsigned sp = 0;; ++sp) {
          asm volatile("" ::: "memory");
          m0 = ld16(ws, l16, so); m1 = ld16(ws, l16, so2);
          if (DBG) ++np1;
          if (__builtin_expect(!__any(max(mx4(m0), mx4(m1)) == SENT), 1)) break;
          if (sp > SPIN_CAP) { ok = false; break; }
        }
      }
      STAMP(14)
      *(LAS v4u*)(Ls) = m0;
      *(LAS v4u*)(Ls + 1024) = m1;
    }
    STAMP(11)
    BARRIER();
    STAMP(12)
    if (DBG) tz = ctime();
    int sbn, ybn;
    auto side = [&]() {
      sbn = sm_base(s + 1, c); ybn = y_base(s + 1, c); PINS(sbn); PINS(ybn);
      int l64 = l16; PIN(l64); l64 <<= 2;
      warm4(wss, lds0 + L_JUNK + w * 512, l64, su + (s < 62 ? 2097152 : 0));
      if (ISA) warm4(wss, lds0 + L_JUNK + w * 512, l64 | oobw, (ybn ^ 8192) + (h * 2 + 1) * 16384 + (w & 1) * 4096);
    };
    if (ISA) {
      {
        v4u a[4];
#pragma unroll
        for (int i = 0; i < 4; ++i) a[i] = lds16(Lm, a_order(i) * 1024);
        SB();
        f4 aq = mkz2(), ak = mkz2();
        a_gemm<0>(W, Lm, Lw0, Lw1, a, wq, wk, aq, ak, idx, side);
        STAMP(1)
        *(LAS v2u*)(Le) = fold2(aq, ak);
      }
      if (w == 0) {
        v4u x0[2], x1_[2];
#pragma unroll
        for (int ks = 0; ks < 2; ++ks) { x0[ks] = lds16(Lm, L_KX + ks * 1024); x1_[ks] = lds16(Lm, L_KX + 4096 + ks * 1024); }
        __builtin_amdgcn_s_setprio(3);
        BARRIER();
        STAMP(2)
        f4 d0 = {0, 0, 0, 0}, d1 = d0, dm = d0;
        v2u q0[2], q1[2], k0[2], k1[2];
#define RD(ks) { q0[ks & 1] = ldstr(Lq, ks * 512); q1[ks & 1] = ldstr(Lq, ks * 512 + 256); k0[ks & 1] = ldstr(Lq, 2048 + ks * 512); k1[ks & 1] = ldstr(Lq, 2048 + ks * 512 + 256); }
#define RX(ks) { x0[ks & 1] = lds16(Lm, L_KX + ks * 1024); x1_[ks & 1] = lds16(Lm, L_KX + 4096 + ks * 1024); }
#define MM(ks) { const v4u q = {q0[ks & 1].x, q0[ks & 1].y, q1[ks & 1].x, q1[ks & 1].y}, km = {k0[ks & 1].x, k0[ks & 1].y, k1[ks & 1].x, k1[ks & 1].y}; \
                 d0 = mfma16(x0[ks & 1], q, d0); d1 = mfma16(x1_[ks & 1], q, d1); dm = mfma16(km, q, dm); PIN(d0); PIN(d1); PIN(dm); }
        RD(0) RD(1) SB(); MM(0) SB(); RD(2) RX(2) SB(); MM(1) SB(); RD(3) RX(3) SB(); MM(2) MM(3) SB();
#undef RD
#undef RX
#undef MM
        STAMP(3)
        const bool t = l16 & 128, gh = l16 & 512;
        float x[5];
#pragma unroll
        for (int r = 0; r < 4; ++r) x[r] = t ? d1[r] : d0[r];
        x[4] = gh ? (t ? dm[3] : dm[2]) : (t ? dm[1] : dm[0]);
        const float cs = 0.08838834764831845f * 1.4426950408889634f;
        const float mxv = xmax(vmax3(vmax3(x[0], x[1], x[2]), x[3], x[4])) * cs;
#pragma unroll
        for (int r = 0; r < 5; ++r) x[r] = __builtin_amdgcn_exp2f(__builtin_fmaf(x[r], cs, -mxv));
        const float inv = __builtin_amdgcn_rcpf(xsum((x[0] + x[1]) + (x[2] + x[3]) + x[4]));
        st8(v2u{cvtpk(x[0] * inv, x[1] * inv), cvtpk(x[2] * inv, x[3] * inv)}, ws, x1, sb);
        const float pm = x[4] * inv, po = bf(__builtin_amdgcn_update_dpp(0u, fb(pm), 0x128, 0xf, 0xf, false));
        st4(cvtpk(pm, po), ws, x2, sb);
        __builtin_amdgcn_s_setprio(0);
        tref = (unsigned)clk() - p.d4;
      } else {
        BARRIER();
        STAMP(2)
        tref = (unsigned)clk();
        if (w >= 4) {
          const v2u k0 = ldstr(Lq, 2048 + (w - 4) * 512), k1 = ldstr(Lq, 2048 + (w - 4) * 512 + 256);
          st16(v4u{k0.x, k0.y, k1.x, k1.y}, ws, x1, (int)WS_KM + s * 65536);
        }
      }
    } else {
      {
        v4u a[4];
#pragma unroll
        for (int i = 0; i < 4; ++i) a[i] = lds16(Lm, i * 1024);
        SB();
        f4 av = mkz2();
        v_gemm<0>(W, Lm, a, av, idx, side);
        STAMP(1)
        auto t0 = __builtin_amdgcn_permlane32_swap(fb(av[0]), fb(av[2]), false, false);
        auto t1 = __builtin_amdgcn_permlane32_swap(fb(av[1]), fb(av[3]), false, false);
        *(LAS unsigned*)(Le) = cvtpk(bf(t0[0]) + bf(t0[1]), bf(t1[0]) + bf(t1[1]));
      }
      f4 y0 = mkz2(), y1 = mkz2(), y2 = mkz2(), y3 = mkz2(); PIN(y0); PIN(y1); PIN(y2); PIN(y3);
      BARRIER();
      STAMP(2)
      v4u va[2];
#pragma unroll
      for (int kb = 0; kb < 2; ++kb) { const v2u a0 = ldstr(Lq, kb * 1024), a1 = ldstr(Lq, kb * 1024 + 64); va[kb] = v4u{a0.x, a0.y, a1.x, a1.y}; }
      v2u R0, R1;
      y1 = smf(va[0], wq, y1, idx); y1 = smf(va[1], wk, y1, idx); PIN(y1); SB();
      tref = (unsigned)clk();
      wq = wl(Lw0, 32768); wk = wl(Lw0, 49152); SB();
      y0 = smf(va[0], W[16], y0, idx); y2 = smf(va[0], W[18], y2, idx); y0 = smf(va[1], W[17], y0, idx); y2 = smf(va[1], W[19], y2, idx); PIN(y0); PIN(y2); SB();
      y3 = smf(va[0], wq, y3, idx); y3 = smf(va[1], wk, y3, idx); PIN(y3); SB();
      wq = wl(Lw1, 0); wk = wl(Lw1, 16384);
      R0 = fold2(y0, y1); PIN(R0); SB();
      y0 = smf(va[0], W[20], mkz2(), idx); y0 = smf(va[1], W[21], y0, idx); PIN(y0);
      R1 = fold2(y2, y3); PIN(R1); SB();
      y1 = smf(va[0], wq, mkz2(), idx); y1 = smf(va[1], wk, y1, idx); PIN(y1); SB();
      wq = wl(Lw1, 32768); wk = wl(Lw1, 49152);
      y2 = smf(va[0], W[22], mkz2(), idx); y2 = smf(va[1], W[23], y2, idx); PIN(y2);
      {
        auto sx = __builtin_amdgcn_permlane16_swap(R0.x, R1.x, false, false); auto sy = __builtin_amdgcn_permlane16_swap(R0.y, R1.y, false, false);
        st16(v4u{sx[0], sy[0], sx[1], sy[1]}, ws, kxo, yb);
        STAMP(3)
      }
      SB();
      y3 = smf(va[0], wq, mkz2(), idx); y3 = smf(va[1], wk, y3, idx); PIN(y3);
      R0 = fold2(y0, y1); PIN(R0); SB();
      R1 = fold2(y2, y3);
      {
        auto sx = __builtin_amdgcn_permlane16_swap(R0.x, R1.x, false, false); auto sy = __builtin_amdgcn_permlane16_swap(R0.y, R1.y, false, false);
        st16(v4u{sx[0], sy[0], sx[1], sy[1]}, ws, kxo, yb + 262144);
      }
    }
    STAMP(4)
    if (s == 63) { if (role == 0 && w == 1) st4(63u, ws, -l16, (int)WS_CTL + C_PROG * 128); break; }
    unsigned fKU;
    {
      v4u u[4];
#pragma unroll
      for (int k = 0; k < 4; ++k) u[k] = ld16(ws, l16, su + k * 1024);
      { int t = l16; PIN(t); const int b = (s + 1) >> 3;
        fKU = __builtin_amdgcn_raw_buffer_load_b32(ws, ISA ? ((t & 16) * ((C_CU + b - (C_FK + b * 32 + h)) * 8)) : 0, (int)WS_CTL + (ISA ? C_FK + b * 32 + h : C_CU + b) * 128, SC1); }
      if (!ISA || w == 1) {
        const v4u ones = mkones();
        if (!ISA) { st16(ones, ws, kxo, ybn); st16(ones, ws, kxo, ybn + 262144); }
        if (w == 1) { st16(ones, ws, x1, sbn); if (role == 0) { int t = l16; PIN(t); st4((unsigned)s, ws, t << 27, (int)WS_CTL + C_PROG * 128); } }
      }
      int sp_ = sb + SM_PX + wq4, sp2 = sp_ + 1024, sy_ = yb + o * 16384 + w * 1024;
      PINS(sp_); PINS(sp2); PINS(sy_);
      STAMP(5)
      spin_until(tref, d2);
      STAMP(6)
      v4u y, p0, p1, pm;
#pragma clang loop unroll(disable)
      for (unsigned sp = 0;; ++sp) {
        asm volatile("" ::: "memory");
        y = ld16(ws, l16, sy_); pm = ld16(ws, pm2, sb); p0 = ld16(ws, l16, sp_); p1 = ld16(ws, l16, sp2);
        if (DBG) ++np;
        if (__builtin_expect(!__any(max(max(mx4(p0), mx4(p1)), max(mx4(pm), mx4(y))) == SENT), 1)) break;
        if (sp > SPIN_CAP) { ok = false; break; }
      }
      STAMP(7)
      f4 acc = mfma16(pm, y, f4{0, 0, 0, 0});
      acc = smf(p0, cat8(u[0], u[1]), acc, idx);
      acc = smf(p1, cat8(u[2], u[3]), acc, idx);
      auto a0 = __builtin_amdgcn_permlane32_swap(fb(acc[0]), fb(acc[2]), false, false);
      auto a1 = __builtin_amdgcn_permlane32_swap(fb(acc[1]), fb(acc[3]), false, false);
      *(LAS float*)(Lr) = bf(a0[0]) + bf(a0[1]); *(LAS float*)(Lr + 64) = bf(a1[0]) + bf(a1[1]);
      BARRIER();
      STAMP(8)
      if (w == 0) {
        f4 t[4];
#pragma unroll
        for (int k = 0; k < 4; ++k) t[k] = *(LAS f4*)(Lr2 + k * 512);
        const f4 sm = (t[0] + t[1]) + (t[2] + t[3]);
        auto b0 = __builtin_amdgcn_permlane32_swap(fb(sm[0]), fb(sm[2]), false, false);
        auto b1 = __builtin_amdgcn_permlane32_swap(fb(sm[1]), fb(sm[3]), false, false);
        st4(cvtpk(bf(b0[0]) + bf(b0[1]), bf(b1[0]) + bf(b1[1])), ws, mo, sb);
        tref = (unsigned)clk() - p.d5;
      } else tref = (unsigned)clk();
    }
    STAMP(9)
    if (__builtin_expect(((s + 1) & 7) == 0, 0)) {
      if (ISA && (unsigned)__builtin_amdgcn_readlane(fKU, 0) < 1u) ok = ok && ctl_wait(ctl, C_FK + ((s + 1) >> 3) * 32 + h, 1u);
      if ((unsigned)__builtin_amdgcn_readlane(fKU, 1) < 256u) ok = ok && ctl_wait(ctl, C_CU + ((s + 1) >> 3), 256u);
    }
    if (ISA) dma16<true>(wss, lds0 + L_KX + w * 1024, kxo, ybn ^ 8192);
    if (DBG) npt += np;
    if (!ok) break;
    sprev = sb; sb = sbn; yb = ybn; su += 2097152;
  }
  WAITV(0);
  BARRIER();
  if (w == 0 && l16 == 0) { if (ok) ctl_add(ctl, C_LAST, 1u); else ctl_st(ctl, C_ABORT, 1u); }
  if (DBG) { if (l16 == 0) { unsigned* d = (unsigned*)(p.ws + WS_DBG) + (role * 8 + w) * 4; d[0] = acc_t / 52; d[1] = np1; d[2] = npt; } }
  if (p.mode & 2) { const unsigned rt1 = rtime(); if (w == 0 && l16 == 0) { unsigned* d = (unsigned*)(p.ws + WS_DBG + 1048576) + role * 4; d[0] = rts; d[1] = rt0; d[2] = rt1; } }
}
#undef l16
#undef idx
#undef x1
#undef x2
#undef pm2
#undef mo
#undef kxo
#undef Le
#undef Lq
#undef Lr
#undef Lr2
#undef Lm
}
namespace work {
using chain::lane_now; using chain::y_base;
constexpr int G_STAGE = 49152, G_W = 32768;
enum { E_FRAG_T = 0, E_FRAG_N = 1, E_OUT = 2 };
struct GemmArgs { v4i rx, rw; int xo, wo, ldw; };
struct Frags { v4u x[4], w[4]; };
template <bool XCOH> DEVI void g_issue1(const GemmArgs& a, int xv, int wv, int w, int t, int st, int i) {
  if (i < 4) dma16<XCOH>(a.rx, st * G_STAGE + (w * 4 + i) * 1024, xv, a.xo + i * 16384 + t * 128);
  else dma16<false>(a.rw, st * G_STAGE + G_W + (w * 2 + i - 4) * 1024, wv, a.wo + (t * 64 + (i - 4) * 4) * a.ldw);
}
DEVI void g_read1(Frags& f, lptr x, const lptr (&wb)[4], int so, int ks, int i) {
  f.x[i] = lds16(x, i * 2048);
  const lptr q = wb[i] + so; const v2u a = ldstr(q, ks * 8192), b = ldstr(q, ks * 8192 + 1024); f.w[i] = v4u{a.x, a.y, b.x, b.y};
}
template <bool TR, bool XCOH, bool RD, bool DMA> DEVI void g_half(f4 (&acc)[4][4], const Frags& c, Frags& n, lptr x0, const lptr (&wb)[4], int so, int ks,
                                                                  const GemmArgs& a, int xv, int wv, int w, int t, int st) {
  const lptr x = (lptr)(((unsigned)(unsigned long long)x0 ^ (ks * 64)) + so);
#pragma unroll
  for (int m = 0; m < 4; ++m) {
#pragma unroll
    for (int j = 0; j < 2; ++j) acc[m][j] = TR ? mfma16(c.w[m], c.x[j], acc[m][j]) : mfma16(c.x[j], c.w[m], acc[m][j]);
    SB();
    if (RD) g_read1(n, x, wb, so, ks, m);
    if (DMA) g_issue1<XCOH>(a, xv, wv, w, t, st, m);
    SB();
#pragma unroll
    for (int j = 2; j < 4; ++j) acc[m][j] = TR ? mfma16(c.w[m], c.x[j], acc[m][j]) : mfma16(c.x[j], c.w[m], acc[m][j]);
    SB();
    if (DMA && m < 2) { g_issue1<XCOH>(a, xv, wv, w, t, st, 4 + m); SB(); }
  }
}
template <bool TR, bool XCOH> DEVI void gemm_main(const GemmArgs& a, f4 (&acc)[4][4], int w) {
  const int lane = lane_now(), g = lane >> 4;
  const int xv = (w * 32 + (lane >> 3)) * 2048 + (((lane & 7) ^ (lane >> 3)) << 4);
  const int wv = (w * 8 + g) * a.ldw + (((lane & 15) ^ ((g | ((w & 1) << 2)) << 1)) << 4);
  const lptr x0 = (lptr)(unsigned)(((w & 3) * 64 + (lane & 15)) * 128 + ((g ^ (lane & 7)) << 4));
  lptr wb[4];
  { const int aa = (lane >> 2) & 3, f = aa | ((g & 1) << 2);
#pragma unroll
    for (int m = 0; m < 4; ++m) wb[m] = (lptr)(unsigned)(G_W + (8 * g + aa) * 256 + ((((w >> 2) * 4 + m) ^ f) << 5) + (lane & 3) * 8); }
#pragma unroll
  for (int t = 0; t < 3; ++t)
#pragma unroll
    for (int i = 0; i < 6; ++i) g_issue1<XCOH>(a, xv, wv, w, t, t, i);
#pragma unroll
  for (int m = 0; m < 4; ++m)
#pragma unroll
    for (int n = 0; n < 4; ++n) acc[m][n] = f4{0, 0, 0, 0};
  Frags f0, f1;
  WAITV(12);
  BARRIER();
#pragma unroll
  for (int i = 0; i < 4; ++i) g_read1(f0, x0, wb, 0, 0, i);
  int st = 0, so = 0;
#pragma clang loop unroll(disable)
  for (int t = 0; t < 13; ++t) {
    g_half<TR, XCOH, true, false>(acc, f0, f1, x0, wb, so, 1, a, xv, wv, w, 0, 0);
    WAITV(6);
    BARRIER();
    const int sn = st == 2 ? 0 : st + 1;
    so = sn * G_STAGE;
    g_half<TR, XCOH, true, true>(acc, f1, f0, x0, wb, so, 0, a, xv, wv, w, t + 3, st);
    st = sn;
  }
  g_half<TR, XCOH, true, false>(acc, f0, f1, x0, wb, G_STAGE, 1, a, xv, wv, w, 0, 0);
  WAITV(6); BARRIER();
  g_half<TR, XCOH, true, false>(acc, f1, f0, x0, wb, 2 * G_STAGE, 0, a, xv, wv, w, 0, 0);
  g_half<TR, XCOH, true, false>(acc, f0, f1, x0, wb, 2 * G_STAGE, 1, a, xv, wv, w, 0, 0);
  WAITV(0); BARRIER();
  g_half<TR, XCOH, true, false>(acc, f1, f0, x0, wb, 0, 0, a, xv, wv, w, 0, 0);
  g_half<TR, XCOH, true, false>(acc, f0, f1, x0, wb, 0, 1, a, xv, wv, w, 0, 0);
  g_half<TR, XCOH, false, false>(acc, f1, f0, x0, wb, 0, 0, a, xv, wv, w, 0, 0);
}
DEVI v4u pack8(f4 a, f4 b, float s) { return v4u{cvtpk(a[0] * s, a[1] * s), cvtpk(a[2] * s, a[3] * s), cvtpk(b[0] * s, b[1] * s), cvtpk(b[2] * s, b[3] * s)}; }
DEVI v4u pack8(f4 a, f4 b) { return v4u{cvtpk(a[0], a[1]), cvtpk(a[2], a[3]), cvtpk(b[0], b[1]), cvtpk(b[2], b[3])}; }
DEVI void publish(unsigned* ctl, int word, int w, unsigned n = 1u) {
  WAITV(0);
  BARRIER();
  if (w == 0 && lane_now() == 0) ctl_add(ctl, word, n);
}
DEVI void unit_qkv_a(const Params& p, int b, int h, int which, int c, int w) {
  GemmArgs a; a.rx = mkrs_s(p.x, N_X); a.rw = mkrs_s(which == 0 ? p.Wq : which == 1 ? p.Wk : p.Wv, N_W); a.xo = b * 256 * 2048; a.wo = h * 256; a.ldw = 8192;
  f4 acc[4][4];
  gemm_main<true, false>(a, acc, w);
  const rsrc_t ws = mkrs(p.ws, WS_END); const int l16 = lane_now() * 16;
#pragma unroll
  for (int n = 0; n < 4; ++n) {
    const int s = b * 8 + (w & 3) * 2 + (n >> 1);
    const int base = (which == 2 ? (int)WS_V + (s * 32 + h) * 8192 : (y_base(s, c) ^ 8192) + (h * 2 + which) * 16384) + (n & 1) * 4096 + (w >> 2) * 2048;
#pragma unroll
    for (int j = 0; j < 2; ++j) st16(pack8(acc[2 * j][n], acc[2 * j + 1][n]), ws, l16, base + j * 1024);
  }
  publish((unsigned*)(p.ws + WS_CTL), (which == 0 ? C_FQ : which == 1 ? C_FK : C_FV) + b * 32 + h, w);
}
DEVI bool await1(unsigned* ctl, int word, unsigned want, int w) {
  bool ok = true;
  if (w == 0) ok = ctl_wait(ctl, word, want);
  BARRIER();
  return ok;
}
DEVI void unit_u(const Params& p, int b, int h, int c0, int nch, int w) {
  unsigned* ctl = (unsigned*)(p.ws + WS_CTL);
  const int lane = lane_now(), g = lane >> 4, l16 = lane * 16;
  const v4i rw = mkrs_s(p.Wo, N_W); const rsrc_t ws = mkrs(p.ws, WS_END);
#pragma clang loop unroll(disable)
  for (int c = 0; c < nch; ++c)
#pragma unroll
    for (int i = 0; i < 4; ++i) {
      const int k = (w * 4 + i) * 4 + g;
      dma16<false>(rw, c * 32768 + (w * 4 + i) * 1024, (w * 16 + g) * 2048 + (((lane & 15) ^ ((k & 7) << 1)) << 4), (h * 128 + i * 4) * 2048 + (c0 + c) * 256);
    }
  await1(ctl, C_FV + b * 32 + h, 1u, w);
  v4u vf[2][4];
#pragma unroll
  for (int t = 0; t < 2; ++t)
#pragma unroll
    for (int ks = 0; ks < 4; ++ks) vf[t][ks] = ld16(ws, l16, (int)WS_V + ((b * 8 + w) * 32 + h) * 8192 + t * 4096 + ks * 1024);
  WAITV(0);
  BARRIER();
  const int kk = 4 * g + ((lane >> 2) & 3);
  unsigned q = kk * 256 + ((kk & 7) << 5) + (lane & 3) * 8;
  int sbase = (int)WS_U + (((b * 8 + w) * 64 + c0 * 8) * 32 + h) * 1024;
  v4u bf_[2][4];
  auto rd = [&](unsigned qq, int ct, v4u (&bb)[4]) {
    const lptr r = (lptr)(qq ^ (ct << 5));
#pragma unroll
    for (int ks = 0; ks < 4; ++ks) { const v2u x = ldstr(r, ks * 8192), y = ldstr(r, ks * 8192 + 4096); bb[ks] = v4u{x.x, x.y, y.x, y.y}; }
  };
  rd(q, 0, bf_[0]);
#pragma clang loop unroll(disable)
  for (int c = 0; c < nch; ++c) {
#pragma unroll
    for (int ct = 0; ct < 8; ++ct) {
      if (ct < 7) rd(q, ct + 1, bf_[(ct + 1) & 1]); else rd(q + 32768, 0, bf_[0]);
      SB();
      f4 d0 = {0, 0, 0, 0}, d1 = d0;
#pragma unroll
      for (int ks = 0; ks < 4; ++ks) { d0 = mfma16(vf[0][ks], bf_[ct & 1][ks], d0); d1 = mfma16(vf[1][ks], bf_[ct & 1][ks], d1); }
      SB();
      st16(pack8(d0, d1), ws, l16, sbase + ct * 32768);
    }
    q += 32768; sbase += 262144;
  }
  publish(ctl, C_CU + b, w, (unsigned)nch);
}
DEVI float gmax(float v) {
  auto a = __builtin_amdgcn_permlane16_swap(fb(v), fb(v), false, false); v = fmaxf(bf(a[0]), bf(a[1]));
  auto b = __builtin_amdgcn_permlane32_swap(fb(v), fb(v), false, false); return fmaxf(bf(b[0]), bf(b[1]));
}
DEVI float gsum(float v) {
  auto a = __builtin_amdgcn_permlane16_swap(fb(v), fb(v), false, false); v = bf(a[0]) + bf(a[1]);
  auto b = __builtin_amdgcn_permlane32_swap(fb(v), fb(v), false, false); return bf(b[0]) + bf(b[1]);
}
DEVI bool chain_ready(unsigned* ctl, int s) { return s < 63 ? ctl_wait(ctl, C_PROG, (unsigned)s + 1u) : ctl_wait(ctl, C_LAST, 64u); }
DEVI void unit_p(const Params& p, int s, int hg, int c, int w) {
  unsigned* ctl = (unsigned*)(p.ws + WS_CTL);
  const int h = hg * 8 + w;
  const rsrc_t ws = mkrs(p.ws, WS_END);
  ctl_wait(ctl, C_FQ + (s >> 3) * 32 + h, 1u); ctl_wait(ctl, C_FK + (s >> 3) * 32 + h, 1u);
  const int lane = lane_now(), g = lane >> 4, l16 = lane * 16;
  const int qb = (y_base(s, c) ^ 8192) + h * 32768;
  v4u qf[2][4], kf[2][4], km[4];
#pragma unroll
  for (int t = 0; t < 2; ++t)
#pragma unroll
    for (int ks = 0; ks < 4; ++ks) { qf[t][ks] = ld16(ws, l16, qb + t * 4096 + ks * 1024); kf[t][ks] = ld16(ws, l16, qb + 16384 + t * 4096 + ks * 1024); }
  if (w == 0) chain_ready(ctl, s);
  BARRIER();
#pragma unroll
  for (int ks = 0; ks < 4; ++ks) km[ks] = ld16(ws, g * 128 + (lane & 7) * 16, (int)WS_KM + (s * 32 + h) * 2048 + ks * 512);
  const float cs = 0.08838834764831845f * 1.4426950408889634f, ninf = -__builtin_inff();
#pragma unroll
  for (int t = 0; t < 2; ++t) {
    f4 d0 = {0, 0, 0, 0}, d1 = d0, dm = d0;
#pragma unroll
    for (int ks = 0; ks < 4; ++ks) { d0 = mfma16(kf[0][ks], qf[t][ks], d0); if (t == 1) d1 = mfma16(kf[1][ks], qf[t][ks], d1); dm = mfma16(km[ks], qf[t][ks], dm); }
    const int lim = (lane & 15) - 4 * g;
    float x[12];
#pragma unroll
    for (int r = 0; r < 4; ++r) {
      x[r] = (t == 1 || r <= lim) ? d0[r] : ninf;
      x[4 + r] = (t == 1 && r <= lim) ? d1[r] : ninf;
      x[8 + r] = g < 2 ? dm[r] : ninf;
    }
    float m = x[0];
#pragma unroll
    for (int r = 1; r < 12; ++r) m = fmaxf(m, x[r]);
    m = gmax(m) * cs;
    float l = 0;
#pragma unroll
    for (int r = 0; r < 12; ++r) { x[r] = __builtin_amdgcn_exp2f(__builtin_fmaf(x[r], cs, -m)); l += x[r]; }
    const float inv = __builtin_amdgcn_rcpf(gsum(l));
    st16(v4u{cvtpk(x[0] * inv, x[1] * inv), cvtpk(x[2] * inv, x[3] * inv), cvtpk(x[4] * inv, x[5] * inv), cvtpk(x[6] * inv, x[7] * inv)}, ws, l16, (int)WS_PX + ((s * 2 + t) * 32 + h) * 1024);
    st8(v2u{cvtpk(x[8] * inv, x[9] * inv), cvtpk(x[10] * inv, x[11] * inv)}, ws, g < 2 ? (lane & 15) * 16 + g * 8 : OOB, (int)WS_PM + ((s * 2 + t) * 8 + (h >> 2)) * 1024 + (h & 3) * 256);
  }
  publish(ctl, C_CP + s, w);
}
DEVI void unit_o(const Params& p, int s, int cq, int c, int w) {
  unsigned* ctl = (unsigned*)(p.ws + WS_CTL);
  const rsrc_t ws = mkrs(p.ws, WS_END);
  const int lane = lane_now(), l16 = lane * 16;
  await1(ctl, C_CU + (s >> 3), 256u, w);
  v4u u[4][4];
#pragma unroll
  for (int ct = 0; ct < 4; ++ct)
#pragma unroll
    for (int k = 0; k < 4; ++k) u[ct][k] = ld16(ws, l16, (int)WS_U + ((s * 64 + cq * 4 + ct) * 32 + w * 4 + k) * 1024);
  await1(ctl, C_CP + s, 4u, w);
  v4u y[4], px[2][4], pm[2];
#pragma unroll
  for (int t = 0; t < 2; ++t) {
#pragma unroll
    for (int k = 0; k < 4; ++k) px[t][k] = ld16(ws, l16, (int)WS_PX + ((s * 2 + t) * 32 + w * 4 + k) * 1024);
    pm[t] = ld16(ws, l16, (int)WS_PM + ((s * 2 + t) * 8 + w) * 1024);
  }
#pragma unroll
  for (int ct = 0; ct < 4; ++ct) y[ct] = ld16(ws, l16, y_base(s, c) + (cq * 4 + ct) * 16384 + w * 1024);
  const lptr d = (lptr)(unsigned)(w * 8192 + l16);
#pragma unroll
  for (int ct = 0; ct < 4; ++ct)
#pragma unroll
    for (int t = 0; t < 2; ++t) {
      f4 a = {0, 0, 0, 0};
#pragma unroll
      for (int k = 0; k < 4; ++k) a = mfma16(u[ct][k], px[t][k], a);
      a = mfma16(y[ct], pm[t], a);
      *(LAS f4*)(d + (ct * 2 + t) * 1024) = a;
    }
  BARRIER();
  const lptr r = (lptr)(unsigned)(w * 1024 + l16);
  f4 t[8];
#pragma unroll
  for (int k = 0; k < 8; ++k) t[k] = *(LAS f4*)(r + k * 8192);
  const f4 sm = ((t[0] + t[1]) + (t[2] + t[3])) + ((t[4] + t[5]) + (t[6] + t[7]));
  __builtin_amdgcn_raw_buffer_store_b64(v2u{cvtpk(sm[0], sm[1]), cvtpk(sm[2], sm[3])}, mkrs(p.out, N_OUT), (lane & 15) * 4096 + (lane >> 4) * 8, (s * 32 + (w & 1) * 16) * 4096 + (cq * 64 + (w >> 1) * 16) * 2, 0);
  BARRIER();
}
DEVI void unit_qkv_b(const Params& p, int b, int cg, int w) {
  GemmArgs a; a.rx = mkrs_s(p.bx, N_X); a.rw = mkrs_s(p.bwqkv, N_BW); a.xo = b * 256 * 2048; a.wo = cg * 256; a.ldw = 6144;
  f4 acc[4][4];
  const rsrc_t ws = mkrs(p.ws, WS_END);
  const int h0 = (cg & 7) * 4 + (w >> 2) * 2;
  if (cg < 16) {
    gemm_main<true, false>(a, acc, w);
    const int l16 = lane_now() * 16;
    const float sc = cg < 8 ? 0.17677669529663687f * 1.4426950408889634f : 1.0f;
    const int base = (cg < 8 ? (int)WS_BQ : (int)WS_BK) + (h0 * 128 + b * 16 + (w & 3) * 4) * 1024;
#pragma unroll
    for (int j = 0; j < 2; ++j)
#pragma unroll
      for (int n = 0; n < 4; ++n) st16(pack8(acc[2 * j][n], acc[2 * j + 1][n], sc), ws, l16, base + j * 131072 + n * 1024);
  } else {
    gemm_main<false, false>(a, acc, w);
    const int l16 = lane_now() * 16;
    const int base = (int)WS_BV + (h0 * 128 + b * 16 + (w & 3) * 4) * 1024;
#pragma unroll
    for (int m = 0; m < 4; ++m)
#pragma unroll
      for (int kb = 0; kb < 2; ++kb) st16(pack8(acc[m][2 * kb], acc[m][2 * kb + 1]), ws, l16, base + (m >> 1) * 131072 + kb * 2048 + (m & 1) * 1024);
  }
  publish((unsigned*)(p.ws + WS_CTL), cg < 8 ? C_BQ + b * 8 + cg : C_BKV + (cg & 7), w);
}
DEVI void unit_out_b(const Params& p, int b, int cg, int w) {
  await1((unsigned*)(p.ws + WS_CTL), C_BO + b, 32u, w);
  GemmArgs a; a.rx = mkrs_s(p.ws + WS_BO, N_X); a.rw = mkrs_s(p.bwo, N_BWO); a.xo = b * 256 * 2048; a.wo = cg * 256; a.ldw = 2048;
  f4 acc[4][4];
  gemm_main<true, true>(a, acc, w);
  const int lane = lane_now(), r16 = lane >> 4;
  const rsrc_t ro = mkrs(p.out, N_OUT);
  const int vo = (lane & 15) * 4096 + (r16 & 1) * 32 + (r16 >> 1) * 16, base = (b * 256 + (w & 3) * 64) * 4096 + 2048 + cg * 256 + (w >> 2) * 128;
#pragma unroll
  for (int j = 0; j < 2; ++j)
#pragma unroll
    for (int n = 0; n < 4; ++n) {
      const v4u v = pack8(acc[2 * j][n], acc[2 * j + 1][n]);
      auto sx = __builtin_amdgcn_permlane16_swap(v.x, v.z, false, false); auto sy = __builtin_amdgcn_permlane16_swap(v.y, v.w, false, false);
      const v4u o = {sx[0], sy[0], sx[1], sy[1]};
      __builtin_amdgcn_raw_buffer_store_b128(o, ro, vo, base + n * 65536 + j * 64, 0); asm volatile("s_nop 1" :: "v"(o) : "memory");
    }
  BARRIER();
}
#define MX3(a, b, c) fmaxf(fmaxf((a), (b)), (c))
struct AttState { f4 o[2][2], l[2], nm[2]; };
DEVI void att_scores(f4 (&sc)[2][2], const AttState& st, const v4u (&q)[2], lptr kp) {
  const v4u k0 = lds16(kp, 0), k1 = lds16(kp, 1024);
#pragma unroll
  for (int t = 0; t < 2; ++t) { sc[t][0] = mfma16(k0, q[t], st.nm[t]); sc[t][1] = mfma16(k1, q[t], st.nm[t]); }
}
template <bool FIRST> DEVI void att_block(f4 (&sc)[2][2], f4 (&nx)[2][2], AttState& st, lptr vp, v4u ones) {
  float mt[2];
#pragma unroll
  for (int t = 0; t < 2; ++t) mt[t] = MX3(MX3(MX3(sc[t][0][0], sc[t][0][1], sc[t][0][2]), sc[t][0][3], sc[t][1][0]), sc[t][1][1], fmaxf(sc[t][1][2], sc[t][1][3]));
  if (FIRST || __builtin_expect(__any(fmaxf(mt[0], mt[1]) > 8.0f), 0)) {
#pragma unroll
    for (int t = 0; t < 2; ++t) {
      float d = gmax(mt[t]); if (!FIRST) d = fmaxf(d, 0.f);
      const float f = __builtin_amdgcn_exp2f(-d);
      st.nm[t] -= d; sc[t][0] -= d; sc[t][1] -= d; nx[t][0] -= d; nx[t][1] -= d;
      if (!FIRST) { st.o[0][t] *= f; st.o[1][t] *= f; st.l[t] *= f; }
    }
  }
  const v4u v0 = lds16(vp, 0), v1 = lds16(vp, 1024);
#pragma unroll
  for (int t = 0; t < 2; ++t) {
    float e[8];
#pragma unroll
    for (int r = 0; r < 4; ++r) { e[r] = __builtin_amdgcn_exp2f(sc[t][0][r]); e[4 + r] = __builtin_amdgcn_exp2f(sc[t][1][r]); }
    const v4u pf = {cvtpk(e[0], e[1]), cvtpk(e[2], e[3]), cvtpk(e[4], e[5]), cvtpk(e[6], e[7])};
    st.o[0][t] = mfma16(v0, pf, st.o[0][t]); st.o[1][t] = mfma16(v1, pf, st.o[1][t]); st.l[t] = mfma16(ones, pf, st.l[t]);
  }
}
DEVI void unit_att_b(const Params& p, int h, int qb, int w) {
  unsigned* ctl = (unsigned*)(p.ws + WS_CTL);
  if (w == 0) { ctl_wait(ctl, C_BKV + (h >> 2), 16u); ctl_wait(ctl, C_BQ + qb * 8 + (h >> 2), 1u); }
  BARRIER();
  const int l16 = lane_now() * 16;
  const v4i rs = mkrs_s(p.ws, WS_END); const rsrc_t ws = mkrs(p.ws, WS_END);
  const int ks = (int)WS_BK + h * 131072 + w * 1024, vs = (int)WS_BV + h * 131072 + w * 1024;
  auto issue = [&](int c) { dma16<true>(rs, (c & 7) * 16384 + w * 1024, l16, ks + c * 8192); dma16<true>(rs, (c & 7) * 16384 + 8192 + w * 1024, l16, vs + c * 8192); };
  v4u q[2];
#pragma unroll
  for (int t = 0; t < 2; ++t) q[t] = ld16(ws, l16, (int)WS_BQ + (h * 128 + qb * 16 + w * 2 + t) * 1024);
  PIN(q[0]); PIN(q[1]);
#pragma unroll
  for (int c = 0; c < 7; ++c) issue(c);
  AttState st;
  const f4 z = {0, 0, 0, 0};
  st.o[0][0] = st.o[0][1] = st.o[1][0] = st.o[1][1] = st.l[0] = st.l[1] = st.nm[0] = st.nm[1] = z;
  v4u ones = {0x3F803F80u, 0x3F803F80u, 0x3F803F80u, 0x3F803F80u}; PIN(ones);
  f4 sa[2][2], sb[2][2];
  WAITV(12);
  BARRIER();
  issue(7);
  { const lptr L0 = (lptr)(unsigned)l16;
    att_scores(sa, st, q, L0);
    att_scores(sb, st, q, L0 + 2048);
    att_block<true>(sa, sb, st, L0 + 8192, ones);
    att_scores(sa, st, q, L0 + 4096);
    att_block<false>(sb, sa, st, L0 + 8192 + 2048, ones);
    att_scores(sb, st, q, L0 + 6144);
    att_block<false>(sa, sb, st, L0 + 8192 + 4096, ones); }
#pragma clang loop unroll(disable)
  for (int c = 1; c < 16; ++c) {
    if (c < 9) WAITV(12); else if (c == 9) WAITV(0);
    if (c <= 9) BARRIER();
    const lptr Lp = (lptr)(unsigned)(l16 + ((c - 1) & 7) * 16384), Lc = (lptr)(unsigned)(l16 + (c & 7) * 16384);
    att_scores(sa, st, q, Lc);
    att_block<false>(sb, sa, st, Lp + 8192 + 6144, ones);
    if (c < 9) { BARRIER(); issue(c + 7); }
    att_scores(sb, st, q, Lc + 2048);
    att_block<false>(sa, sb, st, Lc + 8192, ones);
    att_scores(sa, st, q, Lc + 4096);
    att_block<false>(sb, sa, st, Lc + 8192 + 2048, ones);
    att_scores(sb, st, q, Lc + 6144);
    att_block<false>(sa, sb, st, Lc + 8192 + 4096, ones);
  }
  att_block<false>(sb, sa, st, (lptr)(unsigned)(l16 + 7 * 16384 + 8192 + 6144), ones);
  const int lane = lane_now(), r16 = lane >> 4;
#pragma unroll
  for (int t = 0; t < 2; ++t) {
    const float inv = __builtin_amdgcn_rcpf(st.l[t][0]);
    const v4u v = pack8(st.o[0][t], st.o[1][t], inv);
    auto sx = __builtin_amdgcn_permlane16_swap(v.x, v.z, false, false); auto sy = __builtin_amdgcn_permlane16_swap(v.y, v.w, false, false);
    st16(v4u{sx[0], sy[0], sx[1], sy[1]}, ws, (lane & 15) * 2048 + (r16 & 1) * 32 + (r16 >> 1) * 16, (int)WS_BO + (qb * 256 + w * 32 + t * 16) * 2048 + h * 64);
  }
  publish(ctl, C_BO + qb, w);
}
}

enum { T_QKVA = 0, T_U = 1, T_QKVB = 2, T_ATT = 3, T_OUTB = 4, T_P = 5, T_O = 6 };
constexpr int NUNITS = 768 + 256 + 7 * 64 + 192 + 256 + 64 + 256 + 1024;
struct Table { unsigned short e[NUNITS]; };
constexpr Table make_table() {
  Table t{}; int n = 0;
  auto put = [&](int type, int arg) { t.e[n++] = (unsigned short)(type << 13 | arg); };
  auto kv = [&](int b) { for (int h = 0; h < 32; ++h) put(T_QKVA, b << 7 | 2 << 5 | h); for (int h = 0; h < 32; ++h) put(T_QKVA, b << 7 | 1 << 5 | h); };
  auto q = [&](int b) { for (int h = 0; h < 32; ++h) put(T_QKVA, b << 7 | h); };
  auto u = [&](int b) { if (b == 0) { for (int c = 0; c < 8; ++c) for (int h = 0; h < 32; ++h) put(T_U, 1 << 11 | c << 5 | h); }
                        else for (int c = 0; c < 2; ++c) for (int h = 0; h < 32; ++h) put(T_U, b << 8 | (c * 4) << 5 | h); };
  auto follow = [&](int s0, int s1) { for (int s = s0; s < s1; ++s) { for (int g = 0; g < 4; ++g) put(T_P, s << 2 | g); if (s >= 1) for (int c = 0; c < 16; ++c) put(T_O, (s - 1) << 4 | c); } };
  kv(0); kv(1);
  for (int b = 0; b < 8; ++b) for (int cg = 8; cg < 16; ++cg) put(T_QKVB, b << 5 | cg);
  u(0); u(1);
  for (int b = 0; b < 8; ++b) for (int cg = 16; cg < 24; ++cg) put(T_QKVB, b << 5 | cg);
  kv(2);
  for (int b = 0; b < 8; ++b) for (int cg = 0; cg < 8; ++cg) put(T_QKVB, b << 5 | cg);
  u(2); kv(3); q(0); q(1); u(3);
  for (int i = 0; i < 128; ++i) put(T_ATT, i);
  kv(4); q(2); u(4); kv(5); q(3);
  for (int i = 128; i < 256; ++i) put(T_ATT, i);
  u(5); kv(6); q(4); u(6); kv(7); q(5); q(6); u(7); q(7);
  for (int i = 0; i < 64; ++i) put(T_OUTB, i);
  follow(0, 64);
  for (int c = 0; c < 16; ++c) put(T_O, 63 << 4 | c);
  return t;
}
__device__ const Table g_table = make_table();

__global__ void __launch_bounds__(512, 2) k_main(Params p) {
  extern __shared__ __attribute__((aligned(16))) unsigned char lds[];
  unsigned* ctl = (unsigned*)(p.ws + WS_CTL);
  const int w = rfl(threadIdx.x >> 6);
  volatile LAS int* sh = (volatile LAS int*)(LDS_BYTES - 16);
  const int xc = xcc_id();
  if (threadIdx.x == 0) {
    int tk = 99;
    if (xc < 4) tk = (int)__hip_atomic_fetch_add(ctl + (C_XT + xc) * 32, 1u, __ATOMIC_RELAXED, __HIP_MEMORY_SCOPE_AGENT);
    asm volatile("s_waitcnt vmcnt(0)" ::: "memory");
    ctl_add(ctl, C_ARR, 1u);
    sh[0] = tk;
  }
  BARRIER();
  const int tk = rfl(sh[0]);
  int role = tk < 16 ? (tk & 1) * 32 + xc * 8 + (tk >> 1) : -1;
  BARRIER();
  if (role < 0) {
    const int c = (int)(rfl(ctl_ld(ctl, C_CAL)) & 1);
    int seen = 0;
#pragma clang loop unroll(disable)
    for (;;) {
      if (w == 0) {
        int v = -1;
        if (seen++ == 1 && ctl_wait(ctl, C_ARR, 256u)) {
          int miss = 0;
          for (int x = 0; x < 4; ++x) { const int t = (int)rfl(ctl_ld(ctl, C_XT + x)); miss += t < 16 ? 16 - t : 0; }
          if (miss > 0) {
            int m = 0; if (chain::lane_now() == 0) m = (int)__hip_atomic_fetch_add(ctl + C_FB * 32, 1u, __ATOMIC_RELAXED, __HIP_MEMORY_SCOPE_AGENT);
            m = rfl(m);
            if (m < miss) for (int x = 0; x < 4; ++x) { const int t = (int)rfl(ctl_ld(ctl, C_XT + x)), k = t < 16 ? 16 - t : 0; if (m >= 0 && m < k) { const int q = t + m; v = (1 << 20) | ((q & 1) * 32 + x * 8 + (q >> 1)); } m -= k; }
          }
        }
        if (v < 0) {
          if (chain::lane_now() == 0) v = (int)__hip_atomic_fetch_add(ctl + C_TICKET * 32, 1u, __ATOMIC_RELAXED, __HIP_MEMORY_SCOPE_AGENT);
          v = rfl(v);
        }
        if (chain::lane_now() == 0) sh[1] = v;
      }
      BARRIER();
      int v = rfl(sh[1]);
      BARRIER();
      if (v & (1 << 20)) { role = v & 63; break; }
      if (v >= NUNITS) break;
      const int e = rfl((int)g_table.e[v]), type = e >> 13, a = e & 8191;
      if (type == T_QKVA) work::unit_qkv_a(p, a >> 7, a & 31, (a >> 5) & 3, c, w);
      else if (type == T_U) work::unit_u(p, (a >> 8) & 7, a & 31, (a >> 5) & 7, (a >> 11) ? 1 : 4, w);
      else if (type == T_QKVB) work::unit_qkv_b(p, a >> 5, a & 31, w);
      else if (type == T_ATT) work::unit_att_b(p, a >> 3, a & 7, w);
      else if (type == T_OUTB) work::unit_out_b(p, a >> 3, a & 7, w);
      else if (type == T_P) work::unit_p(p, a >> 2, a & 3, c, w);
      else work::unit_o(p, a >> 4, a & 15, c, w);
    }
  }
  if (role >= 32) chain::run<false>(p, role, (lptr)lds);
  else if (role >= 0) chain::run<true>(p, role, (lptr)lds);
}

__global__ void __launch_bounds__(256) k_init(unsigned char* ws) {
  const int id = blockIdx.x * 256 + threadIdx.x;
  const v4u ones = {SENT, SENT, SENT, SENT};
  *(v4u*)(ws + WS_Y + (size_t)id * 16) = ones;
  if (id < 5120) *(v4u*)(ws + WS_SM + (size_t)id * 16) = ones;
  if (id < C_NWORDS && id != C_CAL) *(unsigned*)(ws + WS_CTL + (size_t)id * 128) = 0u;
  if (blockIdx.x == 255) {
    __shared__ unsigned best[2];
    const int w = rfl(threadIdx.x >> 6);
    if (w < 2) {
      const rsrc_t r = mkrs(ws, WS_END); unsigned b = ~0u;
      for (int i = 0; i < 5; ++i) {
        unsigned long long t0, t1;
        asm volatile("s_waitcnt vmcnt(0)\n\ts_memtime %0\n\ts_waitcnt lgkmcnt(0)" : "=s"(t0) :: "memory");
        st4(0u, r, (threadIdx.x & 15) * 4, (int)WS_SM + 6 * 16384 + w * 8192 + i * 1024);
        asm volatile("s_waitcnt vmcnt(0)\n\ts_memtime %0\n\ts_waitcnt lgkmcnt(0)" : "=s"(t1) :: "memory");
        if (i >= 1) b = min(b, (unsigned)(t1 - t0));
      }
      best[w] = b;
    }
    __syncthreads();
    if (threadIdx.x == 0) *(unsigned*)(ws + WS_CTL + C_CAL * 128) = 2u + ((best[1] < best[0] ? 1u : 0u) ^ (unsigned)(xcc_id() >> 2));
  }
}

extern "C" void kernel_launch(void* const* d_in, const int* in_sizes, int n_in, void* d_out, int out_size, void* d_ws, size_t ws_size, hipStream_t stream) {
  (void)in_sizes; (void)n_in; (void)out_size; (void)ws_size;
  Params p;
  p.x = (const unsigned char*)d_in[0]; p.mem0 = (const unsigned char*)d_in[1]; p.Wq = (const unsigned char*)d_in[2]; p.Wk = (const unsigned char*)d_in[3];
  p.Wv = (const unsigned char*)d_in[4]; p.Wo = (const unsigned char*)d_in[5]; p.bx = (const unsigned char*)d_in[7]; p.bwqkv = (const unsigned char*)d_in[8]; p.bwo = (const unsigned char*)d_in[9];
  p.out = (unsigned char*)d_out;
  p.ws = (unsigned char*)(((size_t)d_ws + (2u << 20) - 1) & ~(size_t)((2u << 20) - 1));
  p.d1 = D1; p.d2 = D2; p.d3 = D3; p.d4 = D4; p.d5 = D5; p.mode = KMODE;
  (void)hipFuncSetAttribute((const void*)k_main, hipFuncAttributeMaxDynamicSharedMemorySize, LDS_BYTES);
  k_init<<<256, 256, 0, stream>>>(p.ws);
  k_main<<<256, 512, LDS_BYTES, stream>>>(p);
}
```

```cpp
#include <hip/hip_runtime.h>
#include <hip/hip_bf16.h>
#pragma clang diagnostic ignored "-Wint-to-pointer-cast"
#define LAS __attribute__((address_space(3)))
#define DEVI __device__ __forceinline__
typedef unsigned v4u __attribute__((ext_vector_type(4)));
typedef unsigned v2u __attribute__((ext_vector_type(2)));
typedef unsigned v8u __attribute__((ext_vector_type(8)));
typedef int v4i __attribute__((ext_vector_type(4)));
typedef float f4 __attribute__((ext_vector_type(4)));
typedef float f2 __attribute__((ext_vector_type(2)));
typedef float f16v __attribute__((ext_vector_type(16)));
typedef short s4 __attribute__((ext_vector_type(4)));
typedef __bf16 bf8 __attribute__((ext_vector_type(8)));
typedef __bf16 bf16x16 __attribute__((ext_vector_type(16)));
typedef __bf16 bf2 __attribute__((ext_vector_type(2)));
typedef __amdgpu_buffer_rsrc_t rsrc_t;
typedef LAS unsigned char* lptr;

constexpr size_t MiB = 1u << 20;
constexpr int SPAN = 4 * MiB, CH = 16384;
constexpr unsigned WS_END = 256 * MiB;
constexpr int RN = 31, RU = 15;
DEVI int y_base(int s, int c) { return (s & RN) * SPAN + c * 8192; }
DEVI int qk_base(int s, int c) { return (32 + (s >> 1)) * SPAN + (s & 1) * 128 * CH + (((s ^ c ^ 1) & 1) << 13); }
DEVI int hi_base(int s, int c) { return s * SPAN + 8192 - c * 8192; }
constexpr int O_KM = 160 * CH, O_SM = 168 * CH, O_PX = 173 * CH, O_PM = 181 * CH, O_BQ = 184 * CH, O_BK = 200 * CH, O_BV = 216 * CH  ,
              O_BO = 184 * CH  , O_CTL = 184 * CH  ;
DEVI int u_base(int s, int o, int c) { return (s & RU) * SPAN + o * 4 * CH + (((o >> 5) ^ c ^ 1) << 13); }
constexpr int WS_BOOT = 63 * SPAN + 255 * CH;
constexpr int B_CAL = 0, B_ARR = 1, B_FB = 2, B_ABORT = 4, B_XT = 8  , B_NWORDS = 16;
constexpr int C_ABORT = 0, C_PROG = 2, C_LAST = 3, C_JUNK = 4, C_FK = 8  , C_FQ = 264  , C_FV = 520  , C_OD = 1024  ,
              C_BKV = 784  , C_BQ = 792  , C_BO = 856  , C_CP = 864  , C_CU = 928  , C_KN = 992  , C_NWORDS = 1088;
constexpr int ctl_off(int w) { return (w >> 6) * CH + (w & 63) * 128; }
constexpr unsigned N_X = 4 * MiB, N_W = 8 * MiB, N_BW = 6 * MiB, N_BWO = 2 * MiB, N_OUT = 8 * MiB, N_MEM0 = 16384;
constexpr unsigned SENT = 0xFFFFFFFFu;
constexpr int SC1 = 16;
constexpr int LDS_BYTES = 160 * 1024;

DEVI rsrc_t mkrs(const void* p, unsigned n) { return __builtin_amdgcn_make_buffer_rsrc((void*)p, 0, n, 0x00020000); }
constexpr int OOB = (int)0x80000000u;
DEVI v4i mkrs_s(const void* p, unsigned n) { unsigned long long a = (unsigned long long)p; v4i r; r.x = (int)(unsigned)a; r.y = (int)(unsigned)(a >> 32) & 0xffff; r.z = (int)n; r.w = 0x00020000; return r; }
DEVI int rfl(int v) { return __builtin_amdgcn_readfirstlane(v); }
DEVI unsigned cvtpk(float lo, float hi) { bf2 r; r.x = (__bf16)lo; r.y = (__bf16)hi; return __builtin_bit_cast(unsigned, r); }
DEVI v4u ld16(rsrc_t r, int vo, int so) { return __builtin_amdgcn_raw_buffer_load_b128(r, vo, so, SC1); }
#define NTAUX 18
DEVI v4u ld16nt(rsrc_t r, int vo, int so) { return __builtin_amdgcn_raw_buffer_load_b128(r, vo, so, NTAUX); }
DEVI void st16(v4u v, rsrc_t r, int vo, int so) { asm volatile("" : "+v"(v)); __builtin_amdgcn_raw_buffer_store_b128(v, r, vo, so, SC1); asm volatile("s_nop 1" :: "v"(v) : "memory"); }
DEVI void st8(v2u v, rsrc_t r, int vo, int so) { __builtin_amdgcn_raw_buffer_store_b64(v, r, vo, so, SC1); }
DEVI void st4(unsigned v, rsrc_t r, int vo, int so) { __builtin_amdgcn_raw_buffer_store_b32(v, r, vo, so, SC1); }
DEVI unsigned umax3(unsigned a, unsigned b, unsigned c) { return max(max(a, b), c); }
DEVI unsigned mx4(v4u v) { return max(umax3(v.x, v.y, v.z), v.w); }
template <bool COH, bool FAST = false> DEVI void dma16(v4i rs, unsigned lds, int vo, int so) {
  if (COH) asm volatile("s_mov_b32 m0, %0\n\ts_nop %c4\n\tbuffer_load_dwordx4 %1, %2, %3 offen sc1 lds" :: "s"(lds), "v"(vo), "s"(rs), "s"(so), "n"(FAST ? 0 : 3) : "memory");
  else asm volatile("s_mov_b32 m0, %0\n\ts_nop %c4\n\tbuffer_load_dwordx4 %1, %2, %3 offen lds" :: "s"(lds), "v"(vo), "s"(rs), "s"(so), "n"(FAST ? 0 : 3) : "memory");
}
DEVI void warm4(v4i rs, unsigned lds, int vo, int so) { asm volatile("s_mov_b32 m0, %0\n\ts_nop 3\n\tbuffer_load_dword %1, %2, %3 offen sc1 lds" :: "s"(lds), "v"(vo), "s"(rs), "s"(so) : "memory"); }
#define WAITV(n) asm volatile("s_waitcnt vmcnt(" #n ")" ::: "memory")
#define WAITL(n) asm volatile("s_waitcnt lgkmcnt(" #n ")" ::: "memory")
#define BARRIER() asm volatile("s_waitcnt lgkmcnt(0)\n\ts_barrier" ::: "memory")
DEVI v4u lds16(lptr p, int off) { return *(LAS v4u*)(p + off); }
DEVI v2u ldstr(lptr p, int off) { return __builtin_bit_cast(v2u, __builtin_amdgcn_ds_read_tr16_b64_v4i16((LAS s4*)(p + off))); }
DEVI f4 mfma16(v4u a, v4u b, f4 c) { return __builtin_amdgcn_mfma_f32_16x16x32_bf16(__builtin_bit_cast(bf8, a), __builtin_bit_cast(bf8, b), c, 0, 0, 0); }
DEVI f16v mfma32(v4u a, v4u b, f16v c) { return __builtin_amdgcn_mfma_f32_32x32x16_bf16(__builtin_bit_cast(bf8, a), __builtin_bit_cast(bf8, b), c, 0, 0, 0); }
DEVI f4 smf(v4u a, v8u b, f4 c, int idx) { return __builtin_amdgcn_smfmac_f32_16x16x64_bf16(__builtin_bit_cast(bf8, a), __builtin_bit_cast(bf16x16, b), c, idx, 0, 0); }
DEVI v8u cat8(v4u a, v4u b) { return __builtin_shufflevector(a, b, 0, 1, 2, 3, 4, 5, 6, 7); }
DEVI unsigned fb(float f) { return __builtin_bit_cast(unsigned, f); }
DEVI float bf(unsigned u) { return __builtin_bit_cast(float, u); }
DEVI unsigned ctl_ld(unsigned* ctl, int w) { return __hip_atomic_load(ctl + ctl_off(w) / 4, __ATOMIC_RELAXED, __HIP_MEMORY_SCOPE_AGENT); }
DEVI void ctl_st(unsigned* ctl, int w, unsigned v) { __hip_atomic_store(ctl + ctl_off(w) / 4, v, __ATOMIC_RELAXED, __HIP_MEMORY_SCOPE_AGENT); }
DEVI void ctl_add(unsigned* ctl, int w, unsigned v) { __hip_atomic_fetch_add(ctl + ctl_off(w) / 4, v, __ATOMIC_RELAXED, __HIP_MEMORY_SCOPE_AGENT); }
constexpr unsigned SPIN_CAP = 1u << 20;
DEVI bool ctl_wait(unsigned* ctl, int w, unsigned want, int ab = C_ABORT) {
#pragma clang loop unroll(disable)
  for (unsigned sp = 0;; ++sp) {
    if ((unsigned)rfl(ctl_ld(ctl, w)) >= want) return true;
    __builtin_amdgcn_s_sleep(4);
    if ((sp & 63u) == 63u && rfl(ctl_ld(ctl, ab))) return false;
    if (sp > SPIN_CAP) { ctl_st(ctl, ab, 1u); return false; }
  }
}
DEVI unsigned* ctl_of(unsigned char* ws, int c) { return (unsigned*)(ws + hi_base(48, c) + O_CTL); }
DEVI int xcc_id() { return (int)(__builtin_amdgcn_s_getreg((3 << 11) | 20) & 0xFu); }
struct Params {
  const unsigned char *x, *mem0, *Wq, *Wk, *Wv, *Wo, *bx, *bwqkv, *bwo;
  unsigned char *out, *ws, *dbg;
  int d1, d2, d3, d4, d5, mode, d6, d7;
};
#define D1 600
#define D2 1300
#define D3 500
#define D4 600
#define D5 200
#define D6 20
#define D7 -100
#define KMODE 0
#define KLEAD 0
#define ULEAD 0
#define WLEAD 1
#define NSTREAM 6
#define SDELAY 1100u
#define UHELP 1
#define NOOD 0
#define YR 0
#define YROT(s) ((s) & YR)
#define PROG_EARLY 1
#define ATTWARM true
#define AV 0
#define NWARM 3
#define STREAM1 1
#define PDELAY 0u
#define ATT_SAFE 0
#define DBG 0
#define S0 8
#define WARM 3
#define WARM_AT 0
namespace chain {
constexpr int NRF = 24;
constexpr int L_MEM = 0, L_KX = 16384, L_QT = 24576, L_KT = 26624, L_RED = 28672, L_WL = 32768, L_JUNK = L_RED;
template <int N> DEVI void waitv() { asm volatile("s_waitcnt vmcnt(%0)" :: "n"(N) : "memory"); }
constexpr int slot_of(int f) { return (f + 8) % 10; }
DEVI int wtr_off(int w, int lane) {
  const int g = lane >> 4, a = (lane >> 2) & 3, r = (a >> 1) | ((a & 1) << 1);
  return (8 * g + r) * 256 + ((w ^ (r | ((g & 1) << 2))) << 5) + (lane & 3) * 8;
}
DEVI v8u wtr(lptr p, int off) {
  const v2u t0 = ldstr(p, off), t1 = ldstr(p, off + 1024), t2 = ldstr(p, off + 8192), t3 = ldstr(p, off + 9216);
  return v8u{t0.x, t0.y, t1.x, t1.y, t2.x, t2.y, t3.x, t3.y};
}

template <bool ISA> DEVI void sub_src(const Params& p, int f, int h, v4i& rs, int& so) {
  if (ISA) { rs = mkrs_s((f & 1) ? p.Wk : p.Wq, N_W); so = ((f >> 1) * 64 * 4096 + h * 128) * 2; }
  else if (f < 16) { rs = mkrs_s(p.Wv, N_W); so = (f * 64 * 4096 + h * 128) * 2; }
  else { rs = mkrs_s(p.Wo, N_W); so = ((h * 128 + (f & 1) * 64) * 1024 + ((f - 16) >> 1) * 128) * 2; }
}
template <bool ISA, int F> DEVI void issue_sub(const Params& p, int h, int w, int lane, unsigned lds0) {
  v4i rs; int so; sub_src<ISA>(p, F, h, rs, so);
  const int stride = (ISA || F < 16) ? 8192 : 2048;
#pragma unroll
  for (int i = 0; i < 2; ++i) {
    const int k = (w * 2 + i) * 4 + (lane >> 4), fk = (k & 3) | (((k >> 3) & 1) << 2);
    dma16<false>(rs, lds0 + slot_of(F) * 16384 + (w * 2 + i) * 1024, k * stride + (((lane & 15) ^ (fk << 1)) << 4), so);
  }
}
template <bool ISA, int F> DEVI void load_step(const Params& p, int h, int w, int lane, lptr Lt, unsigned lds0, v8u (&W)[NRF]) {
  waitv<2 * (F + 8 < 31 ? 8 : 31 - F)>();
  BARRIER();
  if constexpr (F + 9 < 32) issue_sub<ISA, F + 9>(p, h, w, lane, lds0);
  lptr q = Lt; asm volatile("" : "+v"(q)); q += slot_of(F) * 16384; asm volatile("" : "+v"(q));
  W[F] = wtr(q, 0);
  if constexpr (F + 1 < NRF) load_step<ISA, F + 1>(p, h, w, lane, Lt, lds0, W);
}
template <bool ISA, int F> DEVI void issue_pre(const Params& p, int h, int w, int lane, unsigned lds0) {
  issue_sub<ISA, F>(p, h, w, lane, lds0);
  if constexpr (F + 1 < 9) issue_pre<ISA, F + 1>(p, h, w, lane, lds0);
}

DEVI float xmax5(float a, float b, float c, float d, float e) {
  float t, u;
  asm("v_max3_f32 %0, %2, %3, %4\n\tv_max3_f32 %0, %0, %5, %6\n\ts_nop 1\n\tv_max_f32_dpp %0, %0, %0 row_ror:8 row_mask:0xf bank_mask:0xf\n\t"
      "v_mov_b32 %1, %0\n\ts_nop 1\n\tv_permlane16_swap_b32 %0, %1\n\tv_max_f32 %0, %0, %1\n\tv_mov_b32 %1, %0\n\ts_nop 1\n\tv_permlane32_swap_b32 %0, %1\n\tv_max_f32 %0, %0, %1"
      : "=&v"(t), "=&v"(u) : "v"(a), "v"(b), "v"(c), "v"(d), "v"(e));
  return t;
}
DEVI float xsum5(float a, float b, float c, float d, float e) {
  float t, u;
  asm("s_nop 0\n\tv_add_f32 %0, %2, %3\n\tv_add_f32 %1, %4, %5\n\tv_add_f32 %0, %0, %1\n\tv_add_f32 %0, %0, %6\n\ts_nop 1\n\tv_add_f32_dpp %0, %0, %0 row_ror:8 row_mask:0xf bank_mask:0xf\n\t"
      "v_mov_b32 %1, %0\n\ts_nop 1\n\tv_permlane16_swap_b32 %0, %1\n\tv_add_f32 %0, %0, %1\n\tv_mov_b32 %1, %0\n\ts_nop 1\n\tv_permlane32_swap_b32 %0, %1\n\tv_add_f32 %0, %0, %1"
      : "=&v"(t), "=&v"(u) : "v"(a), "v"(b), "v"(c), "v"(d), "v"(e));
  return t;
}
constexpr int SM_PX = 32768, SM_PM = 65536, SM_TS = SM_PM + 4096;

DEVI unsigned rtime() { unsigned long long t; asm volatile("s_memrealtime %0\n\ts_waitcnt lgkmcnt(0)" : "=s"(t) :: "memory"); return (unsigned)t; }
DEVI unsigned ctime() { unsigned long long t; asm volatile("s_memtime %0\n\ts_waitcnt lgkmcnt(0)" : "=s"(t) :: "memory"); return (unsigned)t; }
DEVI void spin_until(unsigned t0, int d) {
#pragma clang loop unroll(disable)
  while (ctime() - t0 < (unsigned)d) {}
}
DEVI int lane_now() { unsigned z = 0; asm volatile("" : "+s"(z)); return (int)__builtin_amdgcn_mbcnt_hi(~0u, __builtin_amdgcn_mbcnt_lo(~0u, z)); }
DEVI int mkidx(int lane) { return 0x4444 + (lane & 1) * 0xAAAA; }
DEVI v4u mkones() { unsigned long long z = ~0ull; asm volatile("" : "+s"(z)); const v2u a = __builtin_bit_cast(v2u, z); return v4u{a.x, a.y, a.x, a.y}; }
DEVI f4 mkz2() { unsigned long long z = 0; asm volatile("" : "+s"(z)); const f2 a = __builtin_bit_cast(f2, z); return f4{a.x, a.y, a.x, a.y}; }
#define PIN(x) asm volatile("" : "+v"(x))
#define SB() __builtin_amdgcn_sched_barrier(0)
constexpr int a_order(int i) { return (i & 3) == 3 ? 12 + (i >> 2) : i - (i >> 2); }
DEVI v8u wl(lptr p, int off) { return cat8(lds16(p, off), lds16(p, off + 1024)); }
template <int I, class F> DEVI void a_gemm(const v8u (&W)[NRF], lptr Lm, lptr Lw0, lptr Lw1, v4u (&a)[4], v8u& wq, v8u& wk, f4& aq, f4& ak, int idx, F&& side) {
  constexpr int kb = a_order(I);
  if constexpr (kb < 12) { aq = smf(a[I & 3], W[2 * kb], aq, idx); ak = smf(a[I & 3], W[2 * kb + 1], ak, idx); }
  else { aq = smf(a[I & 3], wq, aq, idx); ak = smf(a[I & 3], wk, ak, idx); }
  SB();
  if constexpr (I + 4 < 16) a[I & 3] = lds16(Lm, a_order(I + 4) * 1024);
  if constexpr (I == 3) wq = wl(Lw0, 32768);
  if constexpr (I == 4) wk = wl(Lw0, 49152);
  if constexpr (I == 7) wq = wl(Lw1, 0);
  if constexpr (I == 8) wk = wl(Lw1, 16384);
  if constexpr (I == 11) wq = wl(Lw1, 32768);
  if constexpr (I == 12) wk = wl(Lw1, 49152);
  if constexpr (I == 1) side();
  SB();
  if constexpr (I + 1 < 16) a_gemm<I + 1>(W, Lm, Lw0, Lw1, a, wq, wk, aq, ak, idx, side);
}
template <int I> DEVI void v_gemm(const v8u (&W)[NRF], lptr Lm, v4u (&a)[4], f4& av, int idx) {
  av = smf(a[I & 3], W[I], av, idx);
  SB();
  if constexpr (I + 4 < 16) a[I & 3] = lds16(Lm, (I + 4) * 1024);
  SB();
  if constexpr (I + 1 < 16) v_gemm<I + 1>(W, Lm, a, av, idx);
}
DEVI unsigned fold(f4 a) { return cvtpk(a[0] + a[1], a[2] + a[3]); }
DEVI v4u tr4(unsigned d0, unsigned d1, unsigned d2, unsigned d3) {
  auto a = __builtin_amdgcn_permlane16_swap(d0, d1, false, false); auto b = __builtin_amdgcn_permlane16_swap(d2, d3, false, false);
  auto c = __builtin_amdgcn_permlane32_swap(a[0], b[0], false, false); auto d = __builtin_amdgcn_permlane32_swap(a[1], b[1], false, false);
  return v4u{c[0], d[0], c[1], d[1]};
}
#define PINS(x) asm volatile("" : "+s"(x))

template <bool ISA> DEVI void run(const Params& p, int role, int c, lptr L) {
  const int w = rfl(threadIdx.x >> 6), h = role & 31, o = role;
  const unsigned lds0 = (unsigned)(unsigned long long)L;
  unsigned* ctl = ctl_of(p.ws, c);
  const rsrc_t ws = mkrs(p.ws, WS_END); const v4i wss = mkrs_s(p.ws, WS_END);
  const unsigned rts = rtime();
  v8u W[NRF];
  { const int lane = lane_now();
    const lptr Lt = L + wtr_off(w, lane);
    issue_pre<ISA, 0>(p, h, w, lane, lds0);
    load_step<ISA, 0>(p, h, w, lane, Lt, lds0, W);
    WAITV(0);
#pragma unroll
    for (int half = 0; half < 4; ++half) {
      if (half == 0) BARRIER();
      v8u t[2];
      lptr q = Lt; PIN(q); q += L_WL + half * 32768; PIN(q);
#pragma unroll
      for (int k = 0; k < 2; ++k) t[k] = wtr(q, k * 16384);
      BARRIER();
      lptr d = L + w * 2048 + lane * 16; PIN(d); d += L_WL + half * 32768; PIN(d);
#pragma unroll
      for (int k = 0; k < 2; ++k) { *(LAS v4u*)(d + k * 16384) = __builtin_shufflevector(t[k], t[k], 0, 1, 2, 3); *(LAS v4u*)(d + k * 16384 + 1024) = __builtin_shufflevector(t[k], t[k], 4, 5, 6, 7); }
    }
  }
  const unsigned rtw = rtime();
  v8u PA;
  {
    int l16, idx, x1, x2, pm2, kxo; lptr Le, Lq;
    const int lane = lane_now(), g = lane >> 4;
    l16 = lane * 16; idx = mkidx(lane);
    Le = ISA ? L + L_QT + (w * 16 + (lane & 15)) * 16 + g * 4 : L + L_QT + (w * 8 + ((lane & 15) >> 1)) * 32 + g * 8 + (lane & 1) * 2;
    Lq = ISA ? L + L_QT + (4 * g + ((lane >> 2) & 3)) * 16 + (lane & 1) * 8 : L + L_QT + (8 * g + ((lane >> 2) & 3)) * 32 + (lane & 3) * 8;
    const int pxb = SM_PX + (h >> 4) * 16384 + (h & 15) * 512, pmb = SM_PM + h * 128;
    x2 = (lane & 8) ? OOB : pmb + (lane & 7) * 16 + (g & 1) * 8 + (g >> 1) * 4;
    if (w == 0) x1 = pxb + ((lane >> 3) & 1) * 256 + ((lane & 7) * 2 + (g >> 1)) * 16 + (g & 1) * 8;
    else x1 = (lane & 8) ? OOB : (h >> 2) * CH + (h & 3) * 2048 + (w & 3) * 512 + g * 128 + (lane & 7) * 16;
    pm2 = (lane & 1) ? OOB : SM_PM + w * 512 + g * 128 + ((lane & 15) >> 1) * 16;
    const int rho = lane & 15, tau = 16 * ((rho >> 1) & 1) + 4 * ((rho >> 2) & 1) + 2 * (rho >> 3) + (rho & 1);
    kxo = ISA ? (h * 2 + 1) * 16384 + (tau >> 4) * 4096 + (g * 16 + (tau & 15)) * 16
              : ((g & 1) + (lane >> 5) * 4) * 131072 + w * 16384 + h * 256 + (lane & 15) * 16;
    PA = v8u{(unsigned)l16, (unsigned)idx, (unsigned)x1, (unsigned)x2, (unsigned)pm2, (unsigned)(unsigned long long)Lq, (unsigned)kxo, (unsigned)(unsigned long long)Le};
    PIN(PA);
  }
#define l16 ((int)PA[0])
#define idx ((int)PA[1])
#define x1 ((int)PA[2])
#define x2 ((int)PA[3])
#define pm2 ((int)PA[4])
#define kxo ((int)PA[6])
#define Le ((lptr)PA[7])
#define Lq ((lptr)PA[5])
#define Lm ((lptr)PA[0])
  const int wq4 = (w >> 2) * 16384 + (w & 3) * 2048;
  int yb = y_base(0, c);
  const int dsm = hi_base(0, c) + O_SM - yb, dkm = dsm - O_SM + O_KM, du = u_base(0, o, c) + (w >> 1) * CH + (w & 1) * 4096 - yb, cb = hi_base(48, c) + O_CTL;
  unsigned rt0 = rtime();
  unsigned tref = ctime();
  unsigned np1 = 0, npt = 0, tz = 0, acc_t = 0; const int site = p.mode >> 8;
#define STAMP(k) if (DBG) { if (site == k && s >= S0 && s < 60) acc_t += ctime() - tz; }
  const int d2 = !ISA ? p.d2 + p.d3 : w == 0 ? p.d2 - p.d4 : p.d2, d1 = w == 0 ? p.d1 - p.d5 : p.d1;
  int sprev = 0; unsigned ieff = 0, isch = rt0, lprev = 0;
  bool ok = true;
#pragma clang loop unroll(disable)
  for (int s = 0;; ++s) {
    unsigned np = 0;
    PIN(PA);
    lptr Ls; { int t = l16; PIN(t); Ls = (lptr)(unsigned)(t + w * 2048); }
    v8u wq = wl(Ls, L_WL), wk = wl(Ls, L_WL + 16384);
    lptr Lw0 = Ls + L_WL, Lw1 = Lw0 + 65536; PIN(Lw0); PIN(Lw1);
    {
      if (__builtin_expect(s == 0, 0)) {
        int t = l16; PIN(t); const int lane = t >> 4; const rsrc_t r0 = mkrs(p.mem0, N_MEM0); const int vo = (((lane & 15) >> 1) * 1024 + w * 128 + (lane >> 4) * 16) * 2;
        unsigned sel = lane & 1; PIN(sel); sel = (sel << 1 | sel << 9) * 0x10001u + 0x05040100u;
        const v4u a0 = __builtin_amdgcn_raw_buffer_load_b128(r0, vo, 0, 0), a1 = __builtin_amdgcn_raw_buffer_load_b128(r0, vo + 16, 0, 0);
        const v4u b0 = __builtin_amdgcn_raw_buffer_load_b128(r0, vo + 128, 0, 0), b1 = __builtin_amdgcn_raw_buffer_load_b128(r0, vo + 144, 0, 0);
        *(LAS v4u*)(Ls) = v4u{__builtin_amdgcn_perm(a0.y, a0.x, sel), __builtin_amdgcn_perm(a0.w, a0.z, sel), __builtin_amdgcn_perm(a1.y, a1.x, sel), __builtin_amdgcn_perm(a1.w, a1.z, sel)};
        *(LAS v4u*)(Ls + 1024) = v4u{__builtin_amdgcn_perm(b0.y, b0.x, sel), __builtin_amdgcn_perm(b0.w, b0.z, sel), __builtin_amdgcn_perm(b1.y, b1.x, sel), __builtin_amdgcn_perm(b1.w, b1.z, sel)};
        if (ISA) ok = ctl_wait(ctl, C_FK + h, 1u);
        ok = ok && ctl_wait(ctl, C_CU, 32u);
        if (p.mode & 2) rt0 = rtime();
      } else {
        int so = sprev + wq4, so2 = so + 1024; PINS(so); PINS(so2);
        STAMP(10)
        if (DBG) { if (site == 17) { WAITV(0); STAMP(17) } }
#pragma clang loop unroll(disable)
        for (;;) {
          unsigned long long a, b;
          asm volatile("s_memtime %0\n\ts_memrealtime %1\n\ts_waitcnt lgkmcnt(0)" : "=&s"(a), "=&s"(b) :: "memory");
          if ((unsigned)a - tref >= (unsigned)d1 && isch - (unsigned)b - 1u >= 200u) break;
        }
        STAMP(13)
#pragma clang loop unroll(disable)
        for (unsigned sp = 0;; ++sp) {
          asm volatile("" ::: "memory");
          const v4u m0 = ld16(ws, l16, so), m1 = ld16(ws, l16, so2);
          *(LAS v4u*)(Ls) = m0;
          *(LAS v4u*)(Ls + 1024) = m1;
          SB();
          if (sp == 0) { STAMP(14) }
          if (DBG) ++np1;
          if (__builtin_expect(!__any(max(mx4(m0), mx4(m1)) == SENT), 1)) break;
          if (sp > SPIN_CAP) { ok = false; break; }
        }
      }
    }
    STAMP(11)
    BARRIER();
    STAMP(12)
    if (DBG) tz = ctime();
    if (KMODE & 4) { if (role == 0 && w == 2) { const unsigned r = rtime(), cc = ctime(); st4(l16 == 0 ? r : cc, mkrs(p.dbg, 4096), l16 < 32 ? l16 >> 2 : OOB, s * 8); } }
    int sb = yb + dsm, yrot = YROT(s) * 8 * CH; PINS(sb); PINS(yrot);
    if (ISA) {
      {
        v4u a[4];
#pragma unroll
        for (int i = 0; i < 4; ++i) a[i] = lds16(Lm, a_order(i) * 1024);
        SB();
        f4 aq = mkz2(), ak = mkz2();
        a_gemm<0>(W, Lm, Lw0, Lw1, a, wq, wk, aq, ak, idx, [&]() {
          if (PROG_EARLY && role == 0 && w == 1) { int t = l16; PIN(t); st4((unsigned)s, ws, -t, cb + ctl_off(C_PROG)); }
          if (w == 0) { const int kb = qk_base(s, c);
#pragma unroll
            for (int i = 0; i < 8; ++i) dma16<true>(wss, lds0 + L_KX + i * 1024, kxo, kb + (i >> 2) * 128 + (i & 3) * 1024); }
        });
        STAMP(1)
        *(LAS unsigned*)(Le) = fold(aq); *(LAS unsigned*)(Le + 2048) = fold(ak);
      }
      if (w == 0) {
        v4u x0[2], x1_[2];
        WAITV(0);
#pragma unroll
        for (int ks = 0; ks < 2; ++ks) { x0[ks] = lds16(Lm, L_KX + ks * 1024); x1_[ks] = lds16(Lm, L_KX + 4096 + ks * 1024); }
        __builtin_amdgcn_s_setprio(3);
        BARRIER();
        STAMP(2)
        f4 d0 = {0, 0, 0, 0}, d1 = d0, dm = d0;
        v2u q0[2], q1[2], k0[2], k1[2];
#define RD(ks) { q0[ks & 1] = ldstr(Lq, ks * 512); q1[ks & 1] = ldstr(Lq, ks * 512 + 256); k0[ks & 1] = ldstr(Lq, 2048 + ks * 512); k1[ks & 1] = ldstr(Lq, 2048 + ks * 512 + 256); }
#define RX(ks) { x0[ks & 1] = lds16(Lm, L_KX + ks * 1024); x1_[ks & 1] = lds16(Lm, L_KX + 4096 + ks * 1024); }
#define MM(ks) { const v4u q = {q0[ks & 1].x, q0[ks & 1].y, q1[ks & 1].x, q1[ks & 1].y}, km = {k0[ks & 1].x, k0[ks & 1].y, k1[ks & 1].x, k1[ks & 1].y}; \
                 d0 = mfma16(x0[ks & 1], q, d0); d1 = mfma16(x1_[ks & 1], q, d1); dm = mfma16(km, q, dm); PIN(d0); PIN(d1); PIN(dm); }
        RD(0) RD(1) SB(); MM(0) SB(); RD(2) RX(2) SB(); MM(1) SB(); RD(3) RX(3) SB(); MM(2) MM(3) SB();
#undef RD
#undef RX
#undef MM
        STAMP(3)
        const bool t = __builtin_amdgcn_inverse_ballot_w64(0xFF00FF00FF00FF00ull), gh = __builtin_amdgcn_inverse_ballot_w64(0xFFFFFFFF00000000ull);
        float x[5];
#pragma unroll
        for (int r = 0; r < 4; ++r) x[r] = t ? d1[r] : d0[r];
        x[4] = gh ? (t ? dm[3] : dm[2]) : (t ? dm[1] : dm[0]);
        const float cs = 0.08838834764831845f * 1.4426950408889634f;
        const float mxv = xmax5(x[0], x[1], x[2], x[3], x[4]) * cs;
#pragma unroll
        for (int r = 0; r < 5; ++r) x[r] = __builtin_amdgcn_exp2f(__builtin_fmaf(x[r], cs, -mxv));
        float po = bf(__builtin_amdgcn_mov_dpp(fb(x[4]), 0x128, 0xf, 0xf, false)); PIN(po); SB();
        const float inv = __builtin_amdgcn_rcpf(xsum5(x[0], x[1], x[2], x[3], x[4]));
        const v2u px = {cvtpk(x[0] * inv, x[1] * inv), cvtpk(x[2] * inv, x[3] * inv)}; const unsigned pm = cvtpk(x[4] * inv, po * inv);
        st8(px, ws, x1, sb); st4(pm, ws, x2, sb);
        __builtin_amdgcn_s_setprio(0);
        tref = ctime();
      } else {
        BARRIER();
        STAMP(2)
        tref = ctime();
        if (w >= 4) {
          const v2u k0 = ldstr(Lq, 2048 + (w - 4) * 512), k1 = ldstr(Lq, 2048 + (w - 4) * 512 + 256);
          st16(v4u{k0.x, k0.y, k1.x, k1.y}, ws, x1, yb + dkm);
        }
      }
    } else {
      {
        v4u a[4];
#pragma unroll
        for (int i = 0; i < 4; ++i) a[i] = lds16(Lm, i * 1024);
        SB();
        f4 av = mkz2();
        v_gemm<0>(W, Lm, a, av, idx);
        STAMP(1)
        const unsigned v2 = fold(av);
        *(LAS unsigned short*)(Le) = (unsigned short)v2; *(LAS unsigned short*)(Le + 4) = (unsigned short)(v2 >> 16);
      }
      f4 y0 = mkz2(), y1 = mkz2(), y2 = mkz2(), y3 = mkz2(); PIN(y0); PIN(y1); PIN(y2); PIN(y3);
      BARRIER();
      STAMP(2)
      v4u va[2];
#pragma unroll
      for (int kb = 0; kb < 2; ++kb) { const v2u a0 = ldstr(Lq, kb * 1024), a1 = ldstr(Lq, kb * 1024 + 128); va[kb] = v4u{a0.x, a0.y, a1.x, a1.y}; }
      y2 = smf(va[0], wq, y2, idx); y2 = smf(va[1], wk, y2, idx); PIN(y2); SB();
      tref = (unsigned)__builtin_amdgcn_s_memtime();
      wq = wl(Lw0, 32768); wk = wl(Lw0, 49152); SB();
      y0 = smf(va[0], W[16], y0, idx); y1 = smf(va[0], W[18], y1, idx); y0 = smf(va[1], W[17], y0, idx); y1 = smf(va[1], W[19], y1, idx); PIN(y0); PIN(y1); SB();
      unsigned d2_ = fold(y2); y2 = mkz2(); PIN(d2_); PIN(y2); SB();
      y3 = smf(va[0], wq, y3, idx); y3 = smf(va[1], wk, y3, idx); PIN(y3); SB();
      wq = wl(Lw1, 0); wk = wl(Lw1, 16384);
      unsigned d0 = fold(y0), d1 = fold(y1); y0 = mkz2(); y1 = mkz2(); PIN(d0); PIN(d1); PIN(y0); PIN(y1); SB();
      y0 = smf(va[0], W[20], y0, idx); y0 = smf(va[1], W[21], y0, idx); PIN(y0); SB();
      unsigned d3 = fold(y3); y3 = mkz2(); PIN(d3); PIN(y3); SB();
      y2 = smf(va[0], wq, y2, idx); y2 = smf(va[1], wk, y2, idx); PIN(y2); SB();
      wq = wl(Lw1, 32768); wk = wl(Lw1, 49152);
      y1 = smf(va[0], W[22], y1, idx); y1 = smf(va[1], W[23], y1, idx); PIN(y1);
      st16(tr4(d0, d1, d2_, d3), ws, kxo ^ yrot, yb);
      STAMP(3)
      SB();
      y3 = smf(va[0], wq, y3, idx); y3 = smf(va[1], wk, y3, idx); PIN(y3);
      d0 = fold(y0); d2_ = fold(y2); d1 = fold(y1); PIN(d0); PIN(d1); PIN(d2_); SB();
      st16(tr4(d0, d1, d2_, fold(y3)), ws, kxo ^ (yrot ^ 262144), yb);
    }
    STAMP(4)
    if (DBG) { if (site == 18) { WAITV(0); STAMP(18) } }
    if (s == 63) { if (!PROG_EARLY && role == 0 && w == 1) st4(63u, ws, -l16, cb + ctl_off(C_PROG)); break; }
    unsigned fKU;
    {
      v4u u[4];
#pragma unroll
      for (int k = 0; k < 4; ++k) u[k] = ld16(ws, l16, (yb & ((RU + 1) * SPAN - 1)) + du + k * 1024);
      if (!PROG_EARLY && role == 0 && w == 1) { int t = l16; PIN(t); st4((unsigned)s, ws, -t, cb + ctl_off(C_PROG)); }
      { int t = l16; PIN(t); const int b = (s + 1) >> 3;
        fKU = __builtin_amdgcn_raw_buffer_load_b32(ws, t == 32 ? sprev + SM_TS : cb + (ISA && t == 0 ? ctl_off(C_FK + b * 32 + h) : ctl_off(C_CU + s + 1)), 0, SC1); }
      int sp_ = sb + SM_PX + wq4, sp2 = sp_ + 1024, sy_ = yb + ((o * 16384) ^ yrot) + w * 1024;
      int smo = sb + (o >> 5) * 16384 + (o & 31) * 256;
      PINS(sp_); PINS(sp2); PINS(sy_); PINS(smo);
      lptr Lr, Lr2; int mo;
      { int t = l16; PIN(t); Lr = (lptr)(unsigned)(L_RED + w * 512 + (t >> 8) * 128 + (t & 16) * 2 + ((t >> 3) & 24) + ((t >> 3) & 4)); Lr2 = (lptr)(unsigned)(L_RED + (t >> 1)); mo = t >> 2; PIN(Lr); PIN(Lr2); PIN(mo); }
      STAMP(5)
      if (DBG) { if (site == 16) { WAITV(0); STAMP(16) } }
      spin_until(tref, d2);
      STAMP(6)
#pragma clang loop unroll(disable)
      for (unsigned sp = 0;; ++sp) {
        asm volatile("" ::: "memory");
        const v4u y = ld16(ws, l16, sy_), pm = ld16(ws, pm2, sb), p0 = ld16(ws, l16, sp_), p1 = ld16(ws, l16, sp2);
        f4 acc = mfma16(pm, y, f4{0, 0, 0, 0});
        acc = smf(p0, cat8(u[0], u[1]), acc, idx);
        acc = smf(p1, cat8(u[2], u[3]), acc, idx);
        SB();
        unsigned mxv = max(max(mx4(p0), mx4(p1)), max(mx4(pm), mx4(y))); PIN(mxv);
        SB();
        *(LAS float*)(Lr) = acc[0] + acc[1]; *(LAS float*)(Lr + 64) = acc[2] + acc[3];
        SB();
        if (sp == 0) { STAMP(7) }
        if (DBG) ++np;
        if (__builtin_expect(!__any(mxv == SENT), 1)) break;
        if (sp > SPIN_CAP) { ok = false; break; }
      }
      STAMP(15)
      BARRIER();
      STAMP(8)
      if (w == 0) {
        f2 t[8];
#pragma unroll
        for (int k = 0; k < 8; ++k) t[k] = *(LAS f2*)(Lr2 + k * 512);
        const f2 sm = ((t[0] + t[1]) + (t[2] + t[3])) + ((t[4] + t[5]) + (t[6] + t[7]));
        st4(cvtpk(sm.x, sm.y), ws, mo, smo);
        if (role == 0) st4(rtime(), ws, -mo, sb + SM_TS);
        tref = ctime();
      } else tref = ctime();
    }
    STAMP(9)
    {
      const unsigned ts = (unsigned)__builtin_amdgcn_readlane(fKU, 2);
      if (s >= 1) {
        const unsigned len = ts - ieff;
        ieff = (int)(isch - (ts + p.d6)) > 0 ? isch : ts + p.d6;
        isch = ieff + (s >= 2 ? min(len, lprev) + p.d7 : 0u);
        lprev = len;
        if (s < 3 || isch - ts > 400u) isch = ts;
      }
    }
    if (__builtin_expect((unsigned)__builtin_amdgcn_readlane(fKU, 1) < 32u || (ISA && (unsigned)__builtin_amdgcn_readlane(fKU, 0) < 1u), 0)) {
      if (ISA) ok = ok && ctl_wait(ctl, C_FK + ((s + 1) >> 3) * 32 + h, 1u);
      ok = ok && ctl_wait(ctl, C_CU + s + 1, 32u);
    }
    if (DBG) npt += np;
    if (!ok) break;
    sprev = sb; yb = (yb + SPAN) & ((RN + 1) * SPAN - 1);
  }
  WAITV(0);
  BARRIER();
  if (w == 0 && l16 == 0) { if (ok) ctl_add(ctl, C_LAST, 1u); else ctl_st(ctl, C_ABORT, 1u); }
  if (DBG) { if (l16 == 0) { unsigned* d = (unsigned*)(p.dbg + 16384) + (role * 8 + w) * 4; d[0] = acc_t / (60 - S0); d[1] = np1; d[2] = npt; } }
  if (p.mode & 2) { const unsigned rt1 = rtime(); if (w == 0 && l16 == 0) { unsigned* d = (unsigned*)(p.dbg + 1048576) + role * 4; d[0] = rts; d[1] = rt0; d[2] = rt1; d[3] = rtw; } }
}
#undef l16
#undef idx
#undef x1
#undef x2
#undef pm2
#undef kxo
#undef Le
#undef Lq
#undef Lm
}
namespace work {
using chain::lane_now;
typedef const __attribute__((address_space(4))) unsigned* ctab;
DEVI void tlog(const Params& p, int k, ctab list) {
  const unsigned t = chain::rtime(), cc = chain::ctime();
  if (threadIdx.x == 0) { unsigned* d = (unsigned*)(p.dbg + 65536); d[k] = t; d[8192 + k] = cc; }
}
enum { T_GEMM = 0, T_U = 1, T_ATT = 2, T_P = 3, T_O = 4, T_W = 5, T_S = 6, T_END = 7 };
enum { K_Q = 0, K_K = 1, K_V = 2, K_BQ = 3, K_BK = 4, K_OUT = 5, K_BV = 6 };
DEVI v4u pack8(f4 a, f4 b, float s) { return v4u{cvtpk(a[0] * s, a[1] * s), cvtpk(a[2] * s, a[3] * s), cvtpk(b[0] * s, b[1] * s), cvtpk(b[2] * s, b[3] * s)}; }
DEVI v4u pack8(f4 a, f4 b) { return v4u{cvtpk(a[0], a[1]), cvtpk(a[2], a[3]), cvtpk(b[0], b[1]), cvtpk(b[2], b[3])}; }
DEVI void publish(unsigned* ctl, int word, int w, unsigned n = 1u) {
  WAITV(0);
  BARRIER();
  if (w == 0 && lane_now() == 0) ctl_add(ctl, word, n);
}
DEVI float gmax(float v) {
  auto a = __builtin_amdgcn_permlane16_swap(fb(v), fb(v), false, false); v = fmaxf(bf(a[0]), bf(a[1]));
  auto b = __builtin_amdgcn_permlane32_swap(fb(v), fb(v), false, false); return fmaxf(bf(b[0]), bf(b[1]));
}
DEVI float gsum(float v) {
  auto a = __builtin_amdgcn_permlane16_swap(fb(v), fb(v), false, false); v = bf(a[0]) + bf(a[1]);
  auto b = __builtin_amdgcn_permlane32_swap(fb(v), fb(v), false, false); return bf(b[0]) + bf(b[1]);
}
constexpr int G_STAGE = 49152, G_W = 32768;
struct GemmSrc { v4i rx, rw; int xo, xj, wo, ldw, xv, wv; };
struct Frags { v4u x[4], w[4]; };
DEVI GemmSrc g_src(const Params& p, int a, int c, int w, bool live) {
  const int kind = a >> 9, b = (a >> 5) & 7, cg = a & 31;
  GemmSrc s;
  s.rx = mkrs_s(kind < K_BQ ? p.x : kind == K_OUT ? p.ws : p.bx, !live ? 0u : kind == K_OUT ? WS_END : N_X);
  s.rw = mkrs_s(kind == K_Q ? p.Wq : kind == K_K ? p.Wk : kind == K_V ? p.Wv : kind == K_OUT ? p.bwo : p.bwqkv, !live ? 0u : kind < K_BQ ? N_W : kind == K_OUT ? N_BWO : N_BW);
  s.xo = kind == K_OUT ? hi_base(32 + b, c) + O_BO : b * 256 * 2048; s.xj = kind == K_OUT ? 2 * CH : 16384; s.wo = cg * 256 + (kind == K_BK ? 2048 : kind == K_BV ? 4096 : 0); s.ldw = kind < K_BQ ? 8192 : kind == K_OUT ? 2048 : 6144;
  PINS(s.xo); PINS(s.xj); PINS(s.wo); PINS(s.ldw);
  const int lane = lane_now(), g = lane >> 4;
  s.xv = (w * 4 * s.xj + (lane >> 5) * (s.xj >> 1) + ((lane >> 3) & 3) * 2048) + (((lane & 7) ^ (lane >> 3)) << 4);
  s.wv = (w * 8 + g) * s.ldw + (((lane & 15) ^ ((g | ((w & 1) << 2)) << 1)) << 4);
  return s;
}
template <bool FAST = false> DEVI void g_issue1(const GemmSrc& a, int lx, int lw, int i) {
  if (i % 3 < 2) { const int j = i - i / 3; dma16<true, FAST>(a.rx, lx + j * 1024, a.xv, a.xo + j * a.xj); }
  else dma16<false, FAST>(a.rw, lw + (i / 3) * 1024, a.wv, a.wo + (i / 3) * 4 * a.ldw);
}
DEVI v4u g_rdw(const lptr (&wb)[4], int so, int ks, int i) { const lptr q = wb[i] + so; const v2u a = ldstr(q, ks * 8192), b = ldstr(q, ks * 8192 + 1024); return v4u{a.x, a.y, b.x, b.y}; }
template <bool TR> DEVI void g_half(f4 (&acc)[4][4], const Frags& c, Frags& n, lptr x0, const lptr (&wb)[4], int so, int ks, const GemmSrc& a, int lx, int lw, int i0) {
  const lptr x = (lptr)(((unsigned)(unsigned long long)x0 ^ (ks * 64)) + so);
#define GM(m, j) acc[m][j] = TR ? mfma16(c.w[m], c.x[j], acc[m][j]) : mfma16(c.x[j], c.w[m], acc[m][j]);
  GM(0, 0) SB(); n.w[0] = g_rdw(wb, so, ks, 0); SB();
  GM(0, 1) SB(); n.x[0] = lds16(x, 0); n.x[1] = lds16(x, 2048); SB();
  GM(0, 2) GM(0, 3) SB(); g_issue1<true>(a, lx, lw, i0); SB();
  GM(1, 0) SB(); n.x[2] = lds16(x, 4096); n.x[3] = lds16(x, 6144); SB();
  GM(1, 1) SB(); n.w[1] = g_rdw(wb, so, ks, 1); SB();
  GM(1, 2) GM(1, 3) SB(); g_issue1<true>(a, lx, lw, i0 + 1); SB();
  GM(2, 0) SB(); n.w[2] = g_rdw(wb, so, ks, 2); SB();
  GM(2, 1) SB(); n.w[3] = g_rdw(wb, so, ks, 3); SB();
  GM(2, 2) GM(2, 3) SB(); g_issue1<true>(a, lx, lw, i0 + 2); SB();
  GM(3, 0) GM(3, 1) GM(3, 2) GM(3, 3) SB();
#undef GM
}
template <bool TR> DEVI void g_epilogue(const Params& p, const f4 (&acc)[4][4], int a, int c, int w) {
  const int kind = a >> 9, b = (a >> 5) & 7, cg = a & 31;
  const rsrc_t ws = mkrs(p.ws, WS_END);
  const int lane = lane_now(), g = lane >> 4, t16 = lane & 15;
  if (!TR) {
    const int vo = (g >> 1) * 1024 + (g & 1) * 512 + t16 * 16, base = hi_base(cg * 4 + (w >> 2) * 2, c) + O_BV + (b * 2 + ((w >> 1) & 1)) * CH + (w & 1) * 4096;
#pragma unroll
    for (int m = 0; m < 4; ++m)
#pragma unroll
      for (int kb = 0; kb < 2; ++kb) st16(pack8(acc[m][2 * kb], acc[m][2 * kb + 1]), ws, vo, base + (m >> 1) * SPAN + kb * 2048 + (m & 1) * 256);
  } else if (kind == K_OUT) {
    const rsrc_t ro = mkrs(p.out, N_OUT);
    const int vo = t16 * 4096 + (g & 1) * 32 + (g >> 1) * 16, base = (b * 256 + (w & 3) * 64) * 4096 + 2048 + cg * 256 + (w >> 2) * 128;
#pragma unroll
    for (int j = 0; j < 2; ++j)
#pragma unroll
      for (int n = 0; n < 4; ++n) {
        const v4u v = pack8(acc[2 * j][n], acc[2 * j + 1][n]);
        auto sx = __builtin_amdgcn_permlane16_swap(v.x, v.z, false, false); auto sy = __builtin_amdgcn_permlane16_swap(v.y, v.w, false, false);
        v4u o = {sx[0], sy[0], sx[1], sy[1]}; PIN(o);
        __builtin_amdgcn_raw_buffer_store_b128(o, ro, vo, base + n * 65536 + j * 64, 0); asm volatile("s_nop 1" :: "v"(o) : "memory");
      }
  } else {
    int vo, b0, b1, on, oj;
    if (kind < K_BQ) {
      const int s = b * 8 + (w & 3) * 2;
      const int o = (kind == K_V ? 64 + cg : cg * 2 + kind) * CH + (w >> 2) * 2048;
      b0 = qk_base(s, c) + o; b1 = qk_base(s + 1, c) + o;
      vo = lane * 16; on = 4096; oj = 1024;
    } else {
      b0 = hi_base(cg * 4 + (w >> 2) * 2, c) + (kind == K_BQ ? O_BQ : O_BK) + (b * 2 + ((w >> 1) & 1)) * CH + (w & 1) * 4096; b1 = b0 + 2048;
      vo = (g >> 1) * 1024 + (g & 1) * 512 + (kind == K_BQ ? t16 : (t16 & 7) + (t16 >> 3) * 16) * 16; on = kind == K_BQ ? 256 : 128; oj = SPAN;
    }
    if (kind == K_BK) {
#pragma unroll
      for (int j = 0; j < 2; ++j) {
        float m = 0.f;
#pragma unroll
        for (int n = 0; n < 4; ++n) { const f4 a = acc[2 * j][n] * acc[2 * j][n] + acc[2 * j + 1][n] * acc[2 * j + 1][n]; const float q = gsum((a[0] + a[1]) + (a[2] + a[3])); m = q > m || q != q ? q : m; }
        unsigned u = fb(m) & 0x7FFFFFFFu;
        u = max(u, (unsigned)__builtin_amdgcn_mov_dpp(u, 0x128, 0xf, 0xf, false)); u = max(u, (unsigned)__builtin_amdgcn_mov_dpp(u, 0x124, 0xf, 0xf, false));
        u = max(u, (unsigned)__builtin_amdgcn_mov_dpp(u, 0x122, 0xf, 0xf, false)); u = max(u, (unsigned)__builtin_amdgcn_mov_dpp(u, 0x121, 0xf, 0xf, false));
        if (lane == 0) __hip_atomic_fetch_max(ctl_of(p.ws, c) + ctl_off(C_KN + cg * 4 + (w >> 2) * 2 + j) / 4, u, __ATOMIC_RELAXED, __HIP_MEMORY_SCOPE_AGENT);
      }
    }
    const float sc = kind == K_BQ ? 0.17677669529663687f * 1.4426950408889634f : 1.0f;
#pragma unroll
    for (int n = 0; n < 4; ++n)
#pragma unroll
      for (int j = 0; j < 2; ++j) st16(pack8(acc[2 * j][n], acc[2 * j + 1][n], sc), ws, vo, (n < 2 ? b0 : b1) + (n & 1) * on + j * oj);
  }
}
DEVI int g_word(int a) {
  const int kind = a >> 9, b = (a >> 5) & 7, cg = a & 31;
  return kind == K_Q ? C_FQ + b * 32 + cg : kind == K_K ? C_FK + b * 32 + cg : kind == K_V ? C_FV + b * 32 + cg : kind == K_BQ ? C_BQ + b * 8 + cg : kind == K_OUT ? C_JUNK : C_BKV + cg;
}
template <bool TR> DEVI int gemm_run(const Params& p, ctab list, int k, int c, int w) {
  unsigned* ctl = ctl_of(p.ws, c);
  int a = (int)list[k] & 8191;
  if ((a >> 9) == K_OUT) { if (w == 0) ctl_wait(ctl, C_BO + ((a >> 5) & 7), 32u); BARRIER(); }
  const int lane = lane_now(), g = lane >> 4;
  const lptr x0 = (lptr)(unsigned)(((w & 3) * 64 + (lane & 15)) * 128 + ((g ^ (lane & 7)) << 4));
  lptr wb[4];
  { const int aa = (lane >> 2) & 3, f = aa | ((g & 1) << 2);
#pragma unroll
    for (int m = 0; m < 4; ++m) wb[m] = (lptr)(unsigned)(G_W + (8 * g + aa) * 256 + ((((w >> 2) * 4 + m) ^ f) << 5) + (lane & 3) * 8); }
  GemmSrc d = g_src(p, a, c, w, true);
  const int lx0 = w * 4096, lw0 = G_W + w * 2048;
#pragma unroll
  for (int t = 0; t < 2; ++t) {
#pragma unroll
    for (int i = 0; i < 6; ++i) g_issue1(d, lx0 + t * G_STAGE, lw0 + t * G_STAGE, i);
    d.xo += 128; d.wo += 64 * d.ldw;
  }
#pragma unroll
  for (int i = 0; i < 3; ++i) g_issue1(d, lx0 + 2 * G_STAGE, lw0 + 2 * G_STAGE, i);
  Frags f0, f1;
  WAITV(9);
  BARRIER();
#pragma unroll
  for (int i = 0; i < 4; ++i) { f0.x[i] = lds16(x0, i * 2048); f0.w[i] = g_rdw(wb, 0, 0, i); }
  int so = 0, sf = 2 * G_STAGE, pend = -1;
#pragma clang loop unroll(disable)
  for (;;) {
    const int e = (int)list[k + 1];
    const bool more = (e >> 13) == T_GEMM && ((e >> 8) & 1);
    f4 acc[4][4];
#pragma unroll
    for (int m = 0; m < 4; ++m)
#pragma unroll
      for (int n = 0; n < 4; ++n) acc[m][n] = f4{0, 0, 0, 0};
#define KTILE(MID) { g_half<TR>(acc, f0, f1, x0, wb, so, 1, d, lx0 + sf, lw0 + sf, 3); d.xo += 128; d.wo += 64 * d.ldw; MID; \
                     sf = so; so = so == 2 * G_STAGE ? 0 : so + G_STAGE; g_half<TR>(acc, f1, f0, x0, wb, so, 0, d, lx0 + sf, lw0 + sf, 0); }
    KTILE(if (pend >= 0) WAITV(14); else WAITV(6); BARRIER())
    KTILE(WAITV(6); BARRIER(); if (pend >= 0 && w == 0 && lane_now() == 0) ctl_add(ctl, pend, 1u))
#pragma clang loop unroll(disable)
    for (int t = 2; t < 13; ++t) KTILE(WAITV(6); BARRIER())
    KTILE(d = g_src(p, more ? e & 8191 : a, c, w, more); WAITV(6); BARRIER())
#pragma clang loop unroll(disable)
    for (int t = 14; t < 16; ++t) KTILE(WAITV(6); BARRIER())
#undef KTILE
    g_epilogue<TR>(p, acc, a, c, w);
    ++k;
    if (KMODE & 4) tlog(p, k - 1, list);
    if (!more) break;
    pend = g_word(a); a = e & 8191;
  }
  publish(ctl, g_word(a), w);
  return k;
}
constexpr int W_JUNK = 147456;
DEVI bool await1(unsigned* ctl, int word, unsigned want, int w) {
  bool ok = true;
  if (w == 0) ok = ctl_wait(ctl, word, want);
  BARRIER();
  return ok;
}
DEVI void sweep(const unsigned char* b, unsigned n, int i0, int i1, int o, int w, int l16) {
  const v4i r = mkrs_s(b, n);
#pragma clang loop unroll(disable)
  for (int i = i0; i < i1; ++i) { dma16<false>(r, W_JUNK + w * 1024, l16, i * 262144 + o); WAITV(31); }
}
DEVI void unit_stream(const Params& p, int j, int w) {
  const int o = (j * 8 + w) * 1024, l16 = lane_now() * 16;
  const unsigned t0 = chain::rtime();
  if (STREAM1) sweep(p.Wk, N_W, 0, 32, o, w, l16);
  while (chain::rtime() - t0 < SDELAY) __builtin_amdgcn_s_sleep(8);
  sweep(p.bwqkv, N_BW, 0, 24, o, w, l16); sweep(p.bx, N_X, 0, 16, o, w, l16); sweep(p.x, N_X, 4, 8, o, w, l16);
  WAITV(0);
}
DEVI void run_u(const Params& p, int h, int role, int c, int w, int klog) {
  unsigned* ctl = ctl_of(p.ws, c);
  const v4i rw = mkrs_s(p.Wo, N_W), wss = mkrs_s(p.ws, WS_END); const rsrc_t ws = mkrs(p.ws, WS_END);
  const int lane = lane_now(), g = lane >> 4, l16 = lane * 16;
  if (role == 0) { sweep(p.x, N_X, 0, 4, (h * 8 + w) * 1024, w, l16); if (STREAM1) sweep(p.Wv, N_W, 0, 32, (h * 8 + w) * 1024, w, l16); }
  else if (STREAM1) sweep(p.Wq, N_W, 0, 32, (h * 8 + w) * 1024, w, l16);
  v4u wo[8][4];
  {
    const int kk = 4 * g + ((lane >> 2) & 3);
    const unsigned q = w * 16384 + kk * 256 + ((kk & 7) << 5) + (lane & 3) * 8;
#pragma unroll
    for (int half = 0; half < 2; ++half) {
#pragma unroll
      for (int i = 0; i < 16; ++i)
        dma16<false>(rw, w * 16384 + i * 1024, g * 2048 + (((lane & 15) ^ ((((i & 1) * 4 + g) & 7) << 1)) << 4), (h * 128 + half * 64 + i * 4) * 2048 + w * 256);
      WAITV(0);
#pragma unroll
      for (int ct = 0; ct < 8; ++ct)
#pragma unroll
        for (int ks = 0; ks < 2; ++ks) { const lptr r = (lptr)(q ^ (ct << 5)); const v2u x = ldstr(r, ks * 8192), y = ldstr(r, ks * 8192 + 4096); wo[ct][half * 2 + ks] = v4u{x.x, x.y, y.x, y.y}; }
      WAITL(0);
    }
  }
  BARRIER();
  const int uo = w * 32 * CH + (((w >> 2) ^ c ^ 1) << 13) + (h >> 3) * CH + (h & 7) * 1024;
  auto fetch_v = [&](int b) {
#pragma unroll
    for (int i = 0; i < 8; ++i) dma16<true>(wss, (b & 1) * 65536 + i * 8192 + w * 1024, l16, qk_base(b * 8 + i, c) + (64 + h) * CH + w * 1024);
  };
  auto poison = [&](int s) {
    const v4u ones = chain::mkones(); const int yo = y_base(s, c) + (h * 2 + (w >> 2)) * CH + (w & 3) * 2048, v = h + w * 32;
    st16(ones, ws, l16, yo); st16(ones, ws, l16, yo + 1024);
    st16(ones, ws, v < 36 ? l16 : OOB, hi_base(s & RN, c) + O_SM + (v >> 3) * CH + (v & 7) * 1024);
  };
  if (KMODE & 4) tlog(p, 7900 + role * 32 + h, nullptr);
  { const unsigned t0 = chain::rtime(); while (chain::rtime() - t0 < PDELAY) __builtin_amdgcn_s_sleep(8); }
#pragma clang loop unroll(disable)
  for (int s = role; s < 8; s += 2) poison(s);
  if (KMODE & 4) { WAITV(0); tlog(p, 7500 + role * 32 + h, nullptr); }
  await1(ctl, C_FV + h, 1u, w);
  if (KMODE & 4) tlog(p, 7600 + role * 32 + h, nullptr);
  fetch_v(0);
  WAITV(6); BARRIER();
  if (KMODE & 4) tlog(p, 7700 + role * 32 + h, nullptr);
  int pend = -1, have = 0;
  const int send = role ? 8 * UHELP : 64;
#pragma clang loop unroll(disable)
  for (int s = role; s < send; s += s < 8 * UHELP ? 2 : 1) {
    const int b = s >> 3, first = (s & 7) <= role;
    if (first ? s >= 8 : s < 4) {
      WAITV(0); BARRIER();
      if (pend >= 0 && w == 0 && lane == 0) ctl_add(ctl, C_CU + pend, 1u);
      pend = -1;
      if (first) {
        if (have < b) { await1(ctl, C_FV + b * 32 + h, 1u, w); fetch_v(b); have = b; }
        WAITV(0); BARRIER();
      }
      if ((KMODE & 4) && s < 4) tlog(p, 7800 + role * 32 + h, nullptr);
    }
    if (s > RU) { if (w == 0) { if (NOOD) ctl_wait(ctl, C_PROG, s - RU); else ctl_wait(ctl, C_OD + s - RU - 1, 8u); } BARRIER(); }
    const lptr vp = (lptr)(unsigned)((b & 1) * 65536 + (s & 7) * 8192 + l16);
    v4u vf[8];
#pragma unroll
    for (int i = 0; i < 8; ++i) vf[i] = lds16(vp, i * 1024);
    const int ub = (s & RU) * SPAN + uo;
#pragma unroll
    for (int ct = 0; ct < 8; ++ct) {
      f4 d0 = {0, 0, 0, 0}, d1 = d0;
#pragma unroll
      for (int ks = 0; ks < 4; ++ks) { d0 = mfma16(vf[ks], wo[ct][ks], d0); d1 = mfma16(vf[4 + ks], wo[ct][ks], d1); }
      st16(pack8(d0, d1), ws, l16, ub + ct * 4 * CH);
    }
    if (pend >= 0) {
      WAITV(8); BARRIER();
      if (w == 0 && lane == 0) ctl_add(ctl, C_CU + pend, 1u);
    }
    pend = s;
    if (s + 8 < send) poison(s + 8);
    if (role == 0 && b == UHELP - 1) poison(s + 9);
    if ((s & 7) == 4 && role == 0) {
      if (s < 32) sweep(p.x, N_X, b * 2 + 8, b * 2 + 10, (h * 8 + w) * 1024, w, l16);
      else if (s == 36) sweep(p.bwo, N_BWO, 0, 8, (h * 8 + w) * 1024, w, l16);
    }
    if (have == b && (s & 7) >= 2 && (b + 1) * 8 < send) {
      if (w == 0) *(volatile LAS unsigned*)(W_JUNK + 8192) = ctl_ld(ctl, C_FV + (b + 1) * 32 + h);
      BARRIER();
      if (rfl(*(volatile LAS unsigned*)(W_JUNK + 8192))) { fetch_v(b + 1); have = b + 1; }
    }
    if ((KMODE & 4) && (s & 7) >= 6 && role == 0) tlog(p, klog + b, nullptr);
  }
  publish(ctl, C_CU + pend, w);
}
DEVI bool chain_ready(unsigned* ctl, int s) { return s < 63 ? ctl_wait(ctl, C_PROG, (unsigned)s + 1u) : ctl_wait(ctl, C_LAST, 64u); }
DEVI void follow_p(const Params& p, int s0, int step, int hg, int c, int w, int klog) {
  unsigned* ctl = ctl_of(p.ws, c);
  const int h = hg * 8 + w;
  const rsrc_t ws = mkrs(p.ws, WS_END); const v4i wss = mkrs_s(p.ws, WS_END);
  const int lane = lane_now(), g = lane >> 4, l16 = lane * 16;
  const float cs = 0.08838834764831845f * 1.4426950408889634f, ninf = -__builtin_inff();
  const int lim = (lane & 15) - 4 * g;
  int known = -1;
#pragma clang loop unroll(disable)
  for (int s = s0; s < 64; s += step) {
    if ((s >> 3) > known) { known = s >> 3; ctl_wait(ctl, C_FQ + known * 32 + h, 1u); ctl_wait(ctl, C_FK + known * 32 + h, 1u); }
    const int qb = qk_base(s, c) + h * 2 * CH, hb = hi_base(s & RN, c);
    v4u qf[2][4], kf[2][4], km[4];
#pragma unroll
    for (int t = 0; t < 2; ++t)
#pragma unroll
      for (int ks = 0; ks < 4; ++ks) qf[t][ks] = ld16nt(ws, l16, qb + t * 4096 + ks * 1024);
    if (w == 0) chain_ready(ctl, s);
    BARRIER();
#pragma unroll
    for (int i = 0; i < 8; ++i) kf[i >> 2][i & 3] = ld16nt(ws, l16, qb + 16384 + i * 1024);
#pragma unroll
    for (int ks = 0; ks < 4; ++ks) km[ks] = ld16nt(ws, g * 128 + (lane & 7) * 16, hb + O_KM + (h >> 2) * CH + (h & 3) * 2048 + ks * 512);
#pragma unroll
    for (int t = 0; t < 2; ++t) {
      f4 d0 = {0, 0, 0, 0}, d1 = d0, dm = d0;
#pragma unroll
      for (int ks = 0; ks < 4; ++ks) { d0 = mfma16(kf[0][ks], qf[t][ks], d0); if (t == 1) d1 = mfma16(kf[1][ks], qf[t][ks], d1); dm = mfma16(km[ks], qf[t][ks], dm); }
      float x[12];
#pragma unroll
      for (int r = 0; r < 4; ++r) {
        x[r] = (t == 1 || r <= lim) ? d0[r] : ninf;
        x[4 + r] = (t == 1 && r <= lim) ? d1[r] : ninf;
        x[8 + r] = g < 2 ? dm[r] : ninf;
      }
      float m = x[0];
#pragma unroll
      for (int r = 1; r < 12; ++r) m = fmaxf(m, x[r]);
      m = gmax(m) * cs;
      float l = 0;
#pragma unroll
      for (int r = 0; r < 12; ++r) { x[r] = __builtin_amdgcn_exp2f(__builtin_fmaf(x[r], cs, -m)); l += x[r]; }
      const float inv = __builtin_amdgcn_rcpf(gsum(l));
      st16(v4u{cvtpk(x[0] * inv, x[1] * inv), cvtpk(x[2] * inv, x[3] * inv), cvtpk(x[4] * inv, x[5] * inv), cvtpk(x[6] * inv, x[7] * inv)}, ws, l16, hb + O_PX + (t * 4 + hg) * CH + w * 1024);
      st8(v2u{cvtpk(x[8] * inv, x[9] * inv), cvtpk(x[10] * inv, x[11] * inv)}, ws, g < 2 ? (lane & 15) * 16 + g * 8 : OOB, hb + O_PM + t * CH + (h >> 2) * 1024 + (h & 3) * 256);
    }
    publish(ctl, C_CP + s, w);
    if (KMODE & 4) tlog(p, klog + s / step, nullptr);
  }
}
DEVI void follow_o(const Params& p, int k0, int step, int x, int c, int w, int klog) {
  unsigned* ctl = ctl_of(p.ws, c);
  const rsrc_t ws = mkrs(p.ws, WS_END), ro = mkrs(p.out, N_OUT);
  const int lane = lane_now(), l16 = lane * 16;
  const lptr d = (lptr)(unsigned)(w * 16384 + l16), r = (lptr)(unsigned)(w * 2048 + l16);
  v4u ua[4][4], ub[4][4];
  auto load_u = [&](v4u (&u)[4][4], int k, int half) { const int o = u_base(k >> 1, 8 * x + (k & 1) * 32 + half * 4, c) + (w >> 1) * CH + (w & 1) * 4096;
#pragma unroll
    for (int ct = 0; ct < 4; ++ct)
#pragma unroll
      for (int j = 0; j < 4; ++j) u[ct][j] = ld16nt(ws, l16, o + ct * 4 * CH + j * 1024);
  };
#pragma clang loop unroll(disable)
  for (int k = k0; k < 128; k += step) {
    const int s = k >> 1, o0 = 8 * x + (k & 1) * 32;
    if (w == 0) { chain_ready(ctl, s); if (s == 63) ctl_wait(ctl, C_CU + 63, 32u); }
    BARRIER();
    load_u(ua, k, 0); load_u(ub, k, 1);
    v4u y[8], px[2][4], pm[2];
#pragma unroll
    for (int ct = 0; ct < 8; ++ct) y[ct] = ld16nt(ws, l16, y_base(s, c) + ((o0 ^ (YROT(s) * 8)) + ct) * 16384 + w * 1024);
    await1(ctl, C_CP + s, 4u, w);
#pragma unroll
    for (int t = 0; t < 2; ++t) {
#pragma unroll
      for (int j = 0; j < 4; ++j) px[t][j] = ld16(ws, l16, hi_base(s & RN, c) + O_PX + (t * 4 + (w >> 1)) * CH + ((w & 1) * 4 + j) * 1024);
      pm[t] = ld16(ws, l16, hi_base(s & RN, c) + O_PM + t * CH + w * 1024);
    }
    auto mm = [&](const v4u (&u)[4][4], int half) {
#pragma unroll
      for (int ct = 0; ct < 4; ++ct)
#pragma unroll
        for (int t = 0; t < 2; ++t) {
          f4 a = {0, 0, 0, 0};
#pragma unroll
          for (int j = 0; j < 4; ++j) a = mfma16(u[ct][j], px[t][j], a);
          a = mfma16(y[half * 4 + ct], pm[t], a);
          *(LAS f4*)(d + (t * 8 + half * 4 + ct) * 1024) = a;
        }
    };
    mm(ua, 0); mm(ub, 1);
    BARRIER();
    if (w == 0 && lane == 0) ctl_add(ctl, C_OD + s, 1u);
    f4 t[2][8];
#pragma unroll
    for (int j = 0; j < 8; ++j) { t[0][j] = *(LAS f4*)(r + j * 16384); t[1][j] = *(LAS f4*)(r + j * 16384 + 1024); }
    const v4u v = pack8(((t[0][0] + t[0][1]) + (t[0][2] + t[0][3])) + ((t[0][4] + t[0][5]) + (t[0][6] + t[0][7])), ((t[1][0] + t[1][1]) + (t[1][2] + t[1][3])) + ((t[1][4] + t[1][5]) + (t[1][6] + t[1][7])));
    auto sx = __builtin_amdgcn_permlane16_swap(v.x, v.z, false, false); auto sy = __builtin_amdgcn_permlane16_swap(v.y, v.w, false, false);
    v4u o = {sx[0], sy[0], sx[1], sy[1]}; PIN(o);
    __builtin_amdgcn_raw_buffer_store_b128(o, ro, (lane & 15) * 4096 + ((lane >> 4) & 1) * 32 + (lane >> 5) * 16, (s * 32 + (w >> 2) * 16) * 4096 + (o0 + (w & 3) * 2) * 32, 2);
    asm volatile("s_nop 1" :: "v"(o) : "memory");
    if (KMODE & 4) tlog(p, klog + k / step, nullptr);
  }
}
DEVI void run_warm(const Params& p, int x, int j, int c, int w) {
  unsigned* ctl = ctl_of(p.ws, c);
  const v4i wss = mkrs_s(p.ws, WS_END); const int l128 = lane_now() * 128;
#pragma clang loop unroll(disable)
  for (int s = 0; s < 63; ++s) {
    if (w == 0) { ctl_wait(ctl, C_CU + s, 32u); if (s > WLEAD) ctl_wait(ctl, C_PROG, (unsigned)(s - WLEAD)); }
    BARRIER();
#pragma clang loop unroll(disable)
    for (int i = j * 8 + w; i < 72; i += 8 * NWARM)
      warm4(wss, W_JUNK + w * 256, l128, i < 8 ? qk_base(s, c) + ((8 * x + i) * 2 + 1) * CH : u_base(s, 8 * x + ((i >> 2) & 7) + ((i - 8) >> 5) * 32, c) + (i & 3) * CH);
  }
  WAITV(0);
}
struct AttState { f16v o, nm; f4 l; };
#define MX3(a, b, c) fmaxf(fmaxf((a), (b)), (c))
DEVI float max15(const f16v& x) { return MX3(MX3(MX3(x[0], x[1], x[2]), MX3(x[3], x[4], x[5]), MX3(x[6], x[7], x[8])), MX3(x[9], x[10], x[11]), MX3(x[12], x[13], x[14])); }
DEVI void att_move(f16v& sc, AttState& st, bool first) {
  float d = fmaxf(max15(sc), sc[15]);
  auto b = __builtin_amdgcn_permlane32_swap(fb(d), fb(d), false, false); d = fmaxf(bf(b[0]), bf(b[1]));
  if (!first) d = fmaxf(d, 0.f);
  st.nm -= d; sc -= d;
  if (!first) { const float f = __builtin_amdgcn_exp2f(-d); st.o *= f; st.l *= f; }
}
#define EX(i) __builtin_amdgcn_exp2f(sc[i])
template <bool NEXT, bool SAFE> DEVI void att_step(const f16v& sc, f16v& nx, AttState& st, const v4u (&q)[2], v4u (&k)[2], const v4u (&v)[2], v4u (&vn)[2], lptr kp, lptr vp, v4u sel) {
  f4 e0, e1; v4u p0, p1;
  if (NEXT) nx = mfma32(k[0], q[0], st.nm);
  e0 = f4{EX(0), EX(1), EX(2), EX(3)}; PIN(e0); SB();
  if (NEXT) nx = mfma32(k[1], q[1], nx);
  e1 = f4{EX(4), EX(5), EX(6), EX(7)};
  if (NEXT) { k[0] = lds16(kp, 0); k[1] = lds16(kp, 1024); vn[0] = lds16(vp, 0); vn[1] = lds16(vp, 1024); }
  p0 = pack8(e0, e1); PIN(p0); SB();
  st.o = mfma32(v[0], p0, st.o);
  e0 = f4{EX(8), EX(9), EX(10), EX(11)}; PIN(e0); SB();
  st.l = mfma16(sel, p0, st.l);
  e1 = f4{EX(12), EX(13), EX(14), EX(15)};
  p1 = pack8(e0, e1); PIN(p1); SB();
  st.o = mfma32(v[1], p1, st.o);
  float m0 = 0.f, m1 = 0.f;
  if (NEXT && SAFE) { m0 = MX3(MX3(nx[0], nx[1], nx[2]), nx[3], nx[4]); m1 = MX3(MX3(nx[5], nx[6], nx[7]), nx[8], nx[9]); PIN(m0); PIN(m1); }
  SB();
  st.l = mfma16(sel, p1, st.l);
  if (NEXT && SAFE) {
    if (__builtin_expect(__any(MX3(MX3(MX3(m0, nx[10], nx[11]), MX3(m1, nx[12], nx[13]), nx[14]), nx[15], 8.0f) > 8.0f), 0)) att_move(nx, st, false);
  }
  SB();
}
template <bool SAFE> DEVI bool att_pass(const Params& p, int h, int qb, int c, int w, float kn2) {
  const int l16 = lane_now() * 16;
  const v4i rs = mkrs_s(p.ws, WS_END); const rsrc_t ws = mkrs(p.ws, WS_END);
  const int cal = c;
  const int ks = hi_base(h, c) + O_BK + w * 1024, vs = ks - O_BK + O_BV;
  auto issue = [&](int i) { dma16<true>(rs, (i & 7) * 16384 + w * 1024, l16, ks + i * CH); dma16<true>(rs, (i & 7) * 16384 + 8192 + w * 1024, l16, vs + i * CH); };
  v4u q[2];
#pragma unroll
  for (int t = 0; t < 2; ++t) q[t] = ld16(ws, l16, hi_base(h, c) + O_BQ + (qb * 2 + (w >> 2)) * CH + ((w & 3) * 2 + t) * 1024);
  PIN(q[0]); PIN(q[1]);
#pragma unroll
  for (int c = 0; c < 8; ++c) issue(c);
  AttState st;
#pragma unroll
  for (int i = 0; i < 16; ++i) st.o[i] = st.nm[i] = 0.f;
  st.l = f4{0, 0, 0, 0};
  if (!SAFE) {
    float qn = 0.f;
#pragma unroll
    for (int i = 0; i < 8; ++i) { const unsigned u = q[i >> 2][i & 3]; const float a = bf(u << 16), bb = bf(u & 0xFFFF0000u); qn += a * a + bb * bb; }
    auto t = __builtin_amdgcn_permlane32_swap(fb(qn), fb(qn), false, false);
    st.nm -= __builtin_amdgcn_sqrtf((bf(t[0]) + bf(t[1])) * kn2) * 1.02f;
  }
  const unsigned sv = (((l16 >> 8) ^ (l16 >> 6)) & 1) ? 0u : 0x3F803F80u;
  v4u ones = {sv, sv, sv, sv}; PIN(ones);
  f16v sa, sb; v4u k[2], va[2], vb[2];
  WAITV(12);
  BARRIER();
  { const lptr L0 = (lptr)(unsigned)l16;
    sa = mfma32(lds16(L0, 1024), q[1], mfma32(lds16(L0, 0), q[0], st.nm));
    k[0] = lds16(L0, 2048); k[1] = lds16(L0, 3072); va[0] = lds16(L0, 8192); va[1] = lds16(L0, 9216);
    if (SAFE) att_move(sa, st, true); }
#pragma clang loop unroll(disable)
  for (int c = 0; c < 15; ++c) {
    const lptr Lc = (lptr)(unsigned)(l16 + (c & 7) * 16384), Ln = (lptr)(unsigned)(l16 + ((c + 1) & 7) * 16384);
    att_step<true, SAFE>(sa, sb, st, q, k, va, vb, Lc + 4096, Lc + 8192 + 2048, ones);
    att_step<true, SAFE>(sb, sa, st, q, k, vb, va, Lc + 6144, Lc + 8192 + 4096, ones);
    att_step<true, SAFE>(sa, sb, st, q, k, va, vb, Ln, Lc + 8192 + 6144, ones);
    att_step<true, SAFE>(sb, sa, st, q, k, vb, va, Ln + 2048, Ln + 8192, ones);
    if (c < 9) {
      if (c < 8) WAITV(10); else WAITV(0);
      BARRIER();
      if (c < 8) issue(c + 8);
    }
  }
  { const lptr Lc = (lptr)(unsigned)(l16 + 7 * 16384);
    att_step<true, SAFE>(sa, sb, st, q, k, va, vb, Lc + 4096, Lc + 8192 + 2048, ones);
    att_step<true, SAFE>(sb, sa, st, q, k, vb, va, Lc + 6144, Lc + 8192 + 4096, ones);
    att_step<true, SAFE>(sa, sb, st, q, k, va, vb, Lc, Lc + 8192 + 6144, ones);
    att_step<false, SAFE>(sb, sa, st, q, k, vb, va, Lc, Lc, ones); }
  if (!SAFE) {
    volatile LAS unsigned* vote = (volatile LAS unsigned*)(W_JUNK + 4096);
    vote[w] = __any(!(st.l[0] >= 0x1p-30f));
    BARRIER();
    const v4u a = *(volatile LAS v4u*)vote, bb = *(volatile LAS v4u*)(vote + 4);
    if (rfl(a.x | a.y | a.z | a.w | bb.x | bb.y | bb.z | bb.w)) return false;
  }
  const int lane = lane_now();
  const float inv = __builtin_amdgcn_rcpf(st.l[0]);
#pragma unroll
  for (int j = 0; j < 2; ++j) {
    const unsigned a0 = cvtpk(st.o[8 * j] * inv, st.o[8 * j + 1] * inv), a1 = cvtpk(st.o[8 * j + 2] * inv, st.o[8 * j + 3] * inv);
    const unsigned b0 = cvtpk(st.o[8 * j + 4] * inv, st.o[8 * j + 5] * inv), b1 = cvtpk(st.o[8 * j + 6] * inv, st.o[8 * j + 7] * inv);
    auto sx = __builtin_amdgcn_permlane32_swap(a0, b0, false, false); auto sy = __builtin_amdgcn_permlane32_swap(a1, b1, false, false);
    st16(v4u{sx[0], sy[0], sx[1], sy[1]}, ws, ((lane & 31) >> 2) * CH + (lane & 3) * 2048 + (lane >> 5) * 16, hi_base(32 + qb, cal) + O_BO + w * 8 * CH + h * 64 + j * 32);
  }
  return true;
}
DEVI void unit_att_b(const Params& p, int h, int qb, int c, int w) {
  unsigned* ctl = ctl_of(p.ws, c);
  if (w == 0) { ctl_wait(ctl, C_BKV + (h >> 2), 16u); ctl_wait(ctl, C_BQ + qb * 8 + (h >> 2), 1u); }
  BARRIER();
  const float kn2 = bf(ctl_ld(ctl, C_KN + h));
  if (ATT_SAFE || !att_pass<false>(p, h, qb, c, w, kn2)) att_pass<true>(p, h, qb, c, w, 0.f);
  publish(ctl, C_BO + qb, w);
}
}

constexpr int NTAB = 1928;
__device__ const unsigned g_table[NTAB] = {
  256,257,258,259,260,261,262,263,264,265,266,267,268,269,270,271,272,275,278,281,284,287,290,293,
  302,311,320,329,338,347,350,353,356,357,358,359,360,361,362,363,364,365,366,367,368,369,370,371,
  372,375,378,381,384,387,390,393,402,411,420,429,438,447,450,453,456,457,458,459,460,461,462,463,
  464,465,466,467,468,469,470,471,472,475,478,481,484,487,490,493,502,511,520,529,538,547,550,553,
  556,557,558,559,560,561,562,563,564,565,566,567,568,569,570,571,572,575,578,581,584,587,590,593,
  602,611,620,629,638,647,650,653,656,669,682,695,708,721,734,746,758,771,784,797,810,823,836,849,
  862,874,886,898,910,922,934,946,958,960,962,964,966,968,970,972,974,987,1000,1013,1026,1039,1052,1064,
  1076,1089,1102,1115,1128,1141,1154,1167,1180,1192,1204,1216,1228,1240,1252,1264,1276,1278,1280,1282,1284,1286,1288,1290,
  1292,1305,1318,1331,1344,1357,1370,1382,1394,1407,1420,1433,1446,1459,1472,1485,1498,1510,1522,1534,1546,1558,1570,1582,
  1594,1596,1598,1600,1602,1604,1606,1608,1610,1623,1636,1649,1662,1675,1688,1700,1712,1725,1738,1751,1764,1777,1790,1803,
  1816,1828,1840,1852,1864,1876,1888,1900,1912,1914,1916,1918,1920,1922,1924,1926,57344,57344,57344,57344,57344,57344,57344,57344,
  57344,57344,57344,57344,57344,57344,57344,57344,0,24584,57344,1,24648,57344,2,32788,57344,3,32852,57344,4,32916,57344,5,
  32980,57344,6,33044,57344,7,1792,16384,16390,16396,16402,16408,16414,57344,49152,1538,16385,16391,16397,16403,16409,16415,57344,49153,
  1540,16386,16392,16398,16404,16410,16416,57344,49154,1542,16387,16393,16399,16405,16411,16417,57344,49155,1664,16388,16394,16400,16406,16412,
  16418,57344,49156,1696,16389,16395,16401,16407,16413,16419,57344,49157,40960,57344,49158,40961,57344,49159,40962,57344,57344,57344,57344,57344,
  57344,57344,57344,57344,57344,57344,57344,57344,57344,57344,57344,57344,8,24585,57344,9,24649,57344,10,32789,57344,11,32853,57344,
  12,32917,57344,13,32981,57344,14,33045,57344,15,1824,16448,16454,16460,16466,16472,16478,57344,49160,1570,16449,16455,16461,16467,
  16473,16479,57344,49161,1572,16450,16456,16462,16468,16474,16480,57344,49162,1574,16451,16457,16463,16469,16475,16481,57344,49163,1666,16452,
  16458,16464,16470,16476,16482,57344,49164,1698,16453,16459,16465,16471,16477,16483,57344,49165,40960,57344,49166,40961,57344,49167,40962,57344,
  57344,57344,57344,57344,57344,57344,57344,57344,57344,57344,57344,57344,57344,57344,57344,57344,16,24586,57344,17,24650,57344,18,32790,
  57344,19,32854,57344,20,32918,57344,21,32982,57344,22,33046,57344,23,1856,16512,16518,16524,16530,16536,16542,57344,49168,1602,
  16513,16519,16525,16531,16537,16543,57344,49169,1604,16514,16520,16526,16532,16538,16544,57344,49170,1606,16515,16521,16527,16533,16539,16545,
  57344,49171,1668,16516,16522,16528,16534,16540,16546,57344,49172,1700,16517,16523,16529,16535,16541,16547,57344,49173,40960,57344,49174,40961,
  57344,49175,40962,57344,57344,57344,57344,57344,57344,57344,57344,57344,57344,57344,57344,57344,57344,57344,57344,57344,24,24587,57344,25,
  24651,57344,26,32791,57344,27,32855,57344,28,32919,57344,29,32983,57344,30,33047,57344,31,1888,16576,16582,16588,16594,16600,
  16606,57344,49176,1634,16577,16583,16589,16595,16601,16607,57344,49177,1636,16578,16584,16590,16596,16602,16608,57344,49178,1638,16579,16585,
  16591,16597,16603,16609,57344,49179,1670,16580,16586,16592,16598,16604,16610,57344,49180,1702,16581,16587,16593,16599,16605,16611,57344,49181,
  40960,57344,49182,40961,57344,49183,40962,57344,1024,1056,3072,582,870,1414,390,934,966,998,16434,2594,57344,1025,1057,3104,
  583,871,1415,391,935,967,999,16435,2595,57344,1026,1058,3074,64,352,896,3073,160,448,480,16436,2596,57344,1027,
  1059,3106,65,353,897,3105,161,449,481,16437,2597,57344,1028,1060,3076,66,354,898,3075,162,450,482,16438,2598,
  57344,1029,1061,3108,67,355,899,3107,163,451,483,16439,2599,57344,1030,1062,3078,68,356,900,3077,164,452,484,
  16440,57344,1031,1063,3110,69,357,901,3109,165,453,485,16441,57344,512,544,288,1984,1382,2311,1799,1446,1478,1510,
  16426,2562,57344,513,545,289,2016,1383,2343,1831,1447,1479,1511,16427,2563,57344,514,546,290,832,864,1408,384,928,
  960,992,16428,2564,57344,515,547,291,833,865,1409,385,929,961,993,16429,2565,57344,516,548,292,834,866,1410,
  386,930,962,994,16430,2566,57344,517,549,293,835,867,1411,387,931,963,995,16431,2567,57344,518,550,294,836,
  868,1412,388,932,964,996,16432,2592,57344,519,551,295,837,869,1413,389,933,965,997,16433,2593,57344,8224,2048,
  1344,326,358,902,3079,166,454,486,16442,57344,8225,2080,1345,327,359,903,3111,167,455,487,16443,57344,8226,2050,
  1346,1376,2305,1793,1440,1472,1504,16420,16444,57344,8227,2082,1347,1377,2337,1825,1441,1473,1505,16421,16445,57344,8228,2052,
  1348,1378,2307,1795,1442,1474,1506,16422,16446,57344,8229,2084,1349,1379,2339,1827,1443,1475,1507,16423,16447,57344,8230,2054,
  1350,1380,2309,1797,1444,1476,1508,16424,2560,57344,8231,2086,1351,1381,2341,1829,1445,1477,1509,16425,2561,57344,8192,57344,
  8193,57344,8194,57344,8195,57344,8196,57344,8197,57344,8198,57344,8199,57344,1032,1064,3136,590,878,1422,398,942,974,1006,
  16498,2658,57344,1033,1065,3168,591,879,1423,399,943,975,1007,16499,2659,57344,1034,1066,3138,72,360,904,3137,168,
  456,488,16500,2660,57344,1035,1067,3170,73,361,905,3169,169,457,489,16501,2661,57344,1036,1068,3140,74,362,906,
  3139,170,458,490,16502,2662,57344,1037,1069,3172,75,363,907,3171,171,459,491,16503,2663,57344,1038,1070,3142,76,
  364,908,3141,172,460,492,16504,57344,1039,1071,3174,77,365,909,3173,173,461,493,16505,57344,520,552,296,1986,
  1390,2375,1863,1454,1486,1518,16490,2626,57344,521,553,297,2018,1391,2407,1895,1455,1487,1519,16491,2627,57344,522,554,
  298,840,872,1416,392,936,968,1000,16492,2628,57344,523,555,299,841,873,1417,393,937,969,1001,16493,2629,57344,
  524,556,300,842,874,1418,394,938,970,1002,16494,2630,57344,525,557,301,843,875,1419,395,939,971,1003,16495,
  2631,57344,526,558,302,844,876,1420,396,940,972,1004,16496,2656,57344,527,559,303,845,877,1421,397,941,973,
  1005,16497,2657,57344,8232,2112,1352,334,366,910,3143,174,462,494,16506,57344,8233,2144,1353,335,367,911,3175,175,
  463,495,16507,57344,8234,2114,1354,1384,2369,1857,1448,1480,1512,16484,16508,57344,8235,2146,1355,1385,2401,1889,1449,1481,
  1513,16485,16509,57344,8236,2116,1356,1386,2371,1859,1450,1482,1514,16486,16510,57344,8237,2148,1357,1387,2403,1891,1451,1483,
  1515,16487,16511,57344,8238,2118,1358,1388,2373,1861,1452,1484,1516,16488,2624,57344,8239,2150,1359,1389,2405,1893,1453,1485,
  1517,16489,2625,57344,8200,57344,8201,57344,8202,57344,8203,57344,8204,57344,8205,57344,8206,57344,8207,57344,1040,1072,3200,598,
  886,1430,406,950,982,1014,16562,2722,57344,1041,1073,3232,599,887,1431,407,951,983,1015,16563,2723,57344,1042,1074,
  3202,80,368,912,3201,176,464,496,16564,2724,57344,1043,1075,3234,81,369,913,3233,177,465,497,16565,2725,57344,
  1044,1076,3204,82,370,914,3203,178,466,498,16566,2726,57344,1045,1077,3236,83,371,915,3235,179,467,499,16567,
  2727,57344,1046,1078,3206,84,372,916,3205,180,468,500,16568,57344,1047,1079,3238,85,373,917,3237,181,469,501,
  16569,57344,528,560,304,1988,1398,2439,1927,1462,1494,1526,16554,2690,57344,529,561,305,2020,1399,2471,1959,1463,1495,
  1527,16555,2691,57344,530,562,306,848,880,1424,400,944,976,1008,16556,2692,57344,531,563,307,849,881,1425,401,
  945,977,1009,16557,2693,57344,532,564,308,850,882,1426,402,946,978,1010,16558,2694,57344,533,565,309,851,883,
  1427,403,947,979,1011,16559,2695,57344,534,566,310,852,884,1428,404,948,980,1012,16560,2720,57344,535,567,311,
  853,885,1429,405,949,981,1013,16561,2721,57344,8240,2176,1360,342,374,918,3207,182,470,502,16570,57344,8241,2208,
  1361,343,375,919,3239,183,471,503,16571,57344,8242,2178,1362,1392,2433,1921,1456,1488,1520,16548,16572,57344,8243,2210,
  1363,1393,2465,1953,1457,1489,1521,16549,16573,57344,8244,2180,1364,1394,2435,1923,1458,1490,1522,16550,16574,57344,8245,2212,
  1365,1395,2467,1955,1459,1491,1523,16551,16575,57344,8246,2182,1366,1396,2437,1925,1460,1492,1524,16552,2688,57344,8247,2214,
  1367,1397,2469,1957,1461,1493,1525,16553,2689,57344,8208,57344,8209,57344,8210,57344,8211,57344,8212,57344,8213,57344,8214,57344,
  8215,57344,1048,1080,3264,606,894,1438,414,958,990,1022,16626,2786,57344,1049,1081,3296,607,895,1439,415,959,991,
  1023,16627,2787,57344,1050,1082,3266,88,376,920,3265,184,472,504,16628,2788,57344,1051,1083,3298,89,377,921,3297,
  185,473,505,16629,2789,57344,1052,1084,3268,90,378,922,3267,186,474,506,16630,2790,57344,1053,1085,3300,91,379,
  923,3299,187,475,507,16631,2791,57344,1054,1086,3270,92,380,924,3269,188,476,508,16632,57344,1055,1087,3302,93,
  381,925,3301,189,477,509,16633,57344,536,568,312,1990,1406,2503,1991,1470,1502,1534,16618,2754,57344,537,569,313,
  2022,1407,2535,2023,1471,1503,1535,16619,2755,57344,538,570,314,856,888,1432,408,952,984,1016,16620,2756,57344,539,
  571,315,857,889,1433,409,953,985,1017,16621,2757,57344,540,572,316,858,890,1434,410,954,986,1018,16622,2758,
  57344,541,573,317,859,891,1435,411,955,987,1019,16623,2759,57344,542,574,318,860,892,1436,412,956,988,1020,
  16624,2784,57344,543,575,319,861,893,1437,413,957,989,1021,16625,2785,57344,8248,2240,1368,350,382,926,3271,190,
  478,510,16634,57344,8249,2272,1369,351,383,927,3303,191,479,511,16635,57344,8250,2242,1370,1400,2497,1985,1464,1496,
  1528,16612,16636,57344,8251,2274,1371,1401,2529,2017,1465,1497,1529,16613,16637,57344,8252,2244,1372,1402,2499,1987,1466,1498,
  1530,16614,16638,57344,8253,2276,1373,1403,2531,2019,1467,1499,1531,16615,16639,57344,8254,2246,1374,1404,2501,1989,1468,1500,
  1532,16616,2752,57344,8255,2278,1375,1405,2533,2021,1469,1501,1533,16617,2753,57344,8216,57344,8217,57344,8218,57344,8219,57344,
  8220,57344,8221,57344,8222,57344,8223,57344,
};

__global__ void __launch_bounds__(512, 2) k_main(Params p) {
  extern __shared__ __attribute__((aligned(16))) unsigned char lds[];
  using namespace work;
  unsigned* boot = (unsigned*)(p.ws + WS_BOOT);
  const int w = rfl(threadIdx.x >> 6);
  if (w == 0) {
    const int xc = xcc_id() & 7;
    int tk = 0;
    if (lane_now() == 0) {
      tk = (int)__hip_atomic_fetch_add(boot + (B_XT + xc) * 32, 1u, __ATOMIC_RELAXED, __HIP_MEMORY_SCOPE_AGENT);
      asm volatile("s_waitcnt vmcnt(0)" ::: "memory");
      ctl_add(boot, B_ARR, 1u);
    }
    tk = rfl(tk);
    int vid = xc * 32 + tk;
    if (tk >= 32) {
      vid = -1;
      if (ctl_wait(boot, B_ARR, 256u, B_ABORT)) {
        int m = 0; if (lane_now() == 0) m = (int)__hip_atomic_fetch_add(boot + B_FB * 32, 1u, __ATOMIC_RELAXED, __HIP_MEMORY_SCOPE_AGENT);
        m = rfl(m);
        for (int x = 0; x < 8; ++x) { const int t = (int)rfl(ctl_ld(boot, B_XT + x)), k = t < 32 ? 32 - t : 0; if (m >= 0 && m < k) vid = x * 32 + t + m; m -= k; }
      }
    }
    *(volatile LAS int*)lds = vid;
  }
  const int c = (int)(rfl(ctl_ld(boot, B_CAL)) & 1);
  BARRIER();
  const int vid = rfl(*(volatile LAS int*)lds);
  BARRIER();
  if (vid < 0) return;
  if ((KMODE & 2) && vid == 0 && threadIdx.x == 0) *(int*)(p.dbg + 8192) = c;
  if (vid < 128 && (vid & 31) < 16) {
    const int role = (vid & 1) * 32 + (vid >> 5) * 8 + ((vid & 15) >> 1);
    if (role >= 32) chain::run<false>(p, role, c, (lptr)lds); else chain::run<true>(p, role, c, (lptr)lds);
    return;
  }
  const ctab list = (ctab)g_table;
  int k = (int)list[vid];
#pragma clang loop unroll(disable)
  for (;;) {
    const int e = (int)list[k], type = e >> 13, a = e & 8191;
    if (type == T_END) break;
    if (type == T_GEMM) { k = (a >> 9) == K_BV ? gemm_run<false>(p, list, k, c, w) : gemm_run<true>(p, list, k, c, w); continue; }
    if (type == T_U) run_u(p, a & 31, a >> 5, c, w, 7168 + (a & 31) * 8);
    else if (type == T_ATT) unit_att_b(p, a >> 3, a & 7, c, w);
    else if (type == T_P) follow_p(p, a >> 6, (a >> 2) & 15, a & 3, c, w, 4096 + (vid & 7) * 64 + (vid >> 5) * 512);
    else if (type == T_W) run_warm(p, vid >> 5, a, c, w);
    else if (type == T_S) unit_stream(p, a, w);
    else follow_o(p, a >> 6, (a >> 2) & 15, a & 3, c, w, 4096 + (vid & 7) * 64 + (vid >> 5) * 512);
    if (KMODE & 4) tlog(p, k, list);
    ++k;
  }
}

__global__ void __launch_bounds__(1024) k_init(unsigned char* ws) {
  const int id = threadIdx.x, w = rfl(id >> 6);
  __shared__ unsigned best[2];
  if (id < 2) best[id] = ~0u;
  __syncthreads();
  {
    const rsrc_t r = mkrs(ws, WS_END); unsigned b[2] = {~0u, ~0u};
#pragma unroll
    for (int i = 0; i < 6; ++i) {
      unsigned long long t0, t1;
      asm volatile("s_waitcnt vmcnt(0)\n\ts_memtime %0\n\ts_waitcnt lgkmcnt(0)" : "=s"(t0) :: "memory");
      st4(0u, r, (id & 63) < 16 ? (id & 15) * 4 : OOB, ((i ^ w) & 1) * 8192 + w * 512);
      asm volatile("s_waitcnt vmcnt(0)\n\ts_memtime %0\n\ts_waitcnt lgkmcnt(0)" : "=s"(t1) :: "memory");
      if (i >= 2) b[i & 1] = min(b[i & 1], (unsigned)(t1 - t0));
    }
    if ((id & 63) == 0) { atomicMin(&best[w & 1], b[0]); atomicMin(&best[(w & 1) ^ 1], b[1]); }
  }
  __syncthreads();
  const int c = (best[1] < best[0] ? 1 : 0) ^ ((xcc_id() >> 2) & 1);
  ctl_of(ws, c)[ctl_off(id) / 4] = 0u;
  if (id < C_NWORDS - 1024) ctl_of(ws, c)[ctl_off(1024 + id) / 4] = 0u;
  if (id < B_NWORDS) *(unsigned*)(ws + WS_BOOT + id * 128) = id == B_CAL ? (unsigned)c : 0u;
}

extern "C" void kernel_launch(void* const* d_in, const int* in_sizes, int n_in, void* d_out, int out_size, void* d_ws, size_t ws_size, hipStream_t stream) {
  (void)in_sizes; (void)n_in; (void)out_size; (void)ws_size;
  Params p;
  p.x = (const unsigned char*)d_in[0]; p.mem0 = (const unsigned char*)d_in[1]; p.Wq = (const unsigned char*)d_in[2]; p.Wk = (const unsigned char*)d_in[3];
  p.Wv = (const unsigned char*)d_in[4]; p.Wo = (const unsigned char*)d_in[5]; p.bx = (const unsigned char*)d_in[7]; p.bwqkv = (const unsigned char*)d_in[8]; p.bwo = (const unsigned char*)d_in[9];
  p.out = (unsigned char*)d_out;
  p.ws = (unsigned char*)d_ws; p.dbg = p.ws;
  p.d1 = D1; p.d2 = D2; p.d3 = D3; p.d4 = D4; p.d5 = D5; p.mode = KMODE; p.d6 = D6; p.d7 = D7;
  (void)hipFuncSetAttribute((const void*)k_main, hipFuncAttributeMaxDynamicSharedMemorySize, LDS_BYTES);
  k_init<<<1, 1024, 0, stream>>>(p.ws);
  k_main<<<256, 512, LDS_BYTES, stream>>>(p);
}
```
